```python
import torch

def _softmax(x, axis=-1):
    return torch.softmax(x, dim=axis)

def _gelu(x):
    return torch.nn.functional.gelu(x, approximate='tanh')

def _silu(x):
    return torch.nn.functional.silu(x)

def _sigmoid(x):
    return torch.sigmoid(x)

def _layernorm(x, axis=-1, eps=1e-05):
    m = x.mean(dim=axis, keepdim=True)
    v = x.var(dim=axis, keepdim=True, unbiased=False)
    return (x - m) / torch.sqrt(v + eps)

def _scatter_add(tgt, idx, src, axis=0):
    return tgt.clone().index_add_(axis, idx, src)

def _segment_sum(src, seg, n):
    out = torch.zeros((n,) + src.shape[1:], dtype=src.dtype)
    return out.index_add_(0, seg, src)

def _pad(x, pw):
    flat = tuple((v for pair in reversed(pw) for v in pair))
    return torch.nn.functional.pad(x, flat)
kernel_name = 'sequential_preLN__ssm_conv_s4__mixer_gmlp__prompt'

def setup_inputs():

    _w = lambda a: (lambda t: t.to(torch.bfloat16) if t.is_floating_point() else t)(torch.as_tensor(a))

    def _A_setup():
        B, L, D, N = (1, 2048, 576, 128)
        A_re = -(lambda _x: torch.exp(torch.as_tensor(_x)))((lambda *s: torch.randn(*s, dtype=torch.bfloat16) * 6.4)(D, N))
        A_im = torch.pi * (lambda *s: torch.randn(*s, dtype=torch.bfloat16) * 0.05)(D, N)
        B_re = (lambda *s: torch.randn(*s, dtype=torch.bfloat16) * 0.05)(D, N)
        B_im = (lambda *s: torch.randn(*s, dtype=torch.bfloat16) * 0.05)(D, N)
        C_re = (lambda *s: torch.randn(*s, dtype=torch.bfloat16) * 0.05)(D, N)
        C_im = (lambda *s: torch.randn(*s, dtype=torch.bfloat16) * 0.05)(D, N)
        log_dt = (lambda *s: torch.randn(*s, dtype=torch.bfloat16) * 0.05)(D) * 0.5 - 4.0
        Dskip = (lambda *s: torch.randn(*s, dtype=torch.bfloat16) * 0.05)(D)
        return {'u': _w((lambda *s: torch.randn(*s, dtype=torch.bfloat16) * 0.05)(B, L, D)), 'A_re': _w(A_re), 'A_im': _w(A_im), 'B_re': _w(B_re), 'B_im': _w(B_im), 'C_re': _w(C_re), 'C_im': _w(C_im), 'log_dt': _w(log_dt), 'Dskip': _w(Dskip)}
    _A_in = _A_setup()

    def _B_setup():
        B, S, D, F = (1, 2048, 576, 4608)
        Fh = F // 2
        return {'x': _w((lambda *s: torch.randn(*s, dtype=torch.bfloat16) * 0.05)(B, S, D)), 'w_in': _w((lambda *s: torch.randn(*s, dtype=torch.bfloat16) * 0.4)(D, 2 * Fh)), 'w_sp': _w(0.01 * (lambda *s: torch.randn(*s, dtype=torch.bfloat16) * 0.4)(S, S)), 'b_sp': _w(1.0 + 0.01 * (lambda *s: torch.randn(*s, dtype=torch.bfloat16) * 0.05)(S)), 'w_out': _w((lambda *s: torch.randn(*s, dtype=torch.bfloat16) * 0.05)(Fh, D))}
    _B_in = _B_setup()
    return {'A_u': _A_in['u'], 'A_A_re': _A_in['A_re'], 'A_A_im': _A_in['A_im'], 'A_B_re': _A_in['B_re'], 'A_B_im': _A_in['B_im'], 'A_C_re': _A_in['C_re'], 'A_C_im': _A_in['C_im'], 'A_log_dt': _A_in['log_dt'], 'A_Dskip': _A_in['Dskip'], 'B_x': _B_in['x'], 'B_w_in': _B_in['w_in'], 'B_w_sp': _B_in['w_sp'], 'B_b_sp': _B_in['b_sp'], 'B_w_out': _B_in['w_out']}

def reference(A_u, A_A_re, A_A_im, A_B_re, A_B_im, A_C_re, A_C_im, A_log_dt, A_Dskip, B_x, B_w_in, B_w_sp, B_b_sp, B_w_out):

    def _A_ref(u, A_re, A_im, B_re, B_im, C_re, C_im, log_dt, Dskip):
        Bsz, L, D = u.shape
        dt = torch.exp(log_dt)[:, None]
        A = A_re + 1j * A_im
        Bc = B_re + 1j * B_im
        Cc = C_re + 1j * C_im
        logdA = dt * A
        dB = dt * Bc
        l = (lambda x, dt: x.to(dt))(torch.arange(L), A_re.dtype)
        P = torch.exp(logdA[:, :, None] * l[None, None, :])
        K = torch.einsum('dn,dnl,dn->dl', Cc, P, dB).real
        L2 = 2 * L
        Kp = _pad(K, ((0, 0), (0, L)))
        up = (lambda x, *d: x.permute(*d))(u, 0, 2, 1)
        up = _pad(up, ((0, 0), (0, 0), (0, L)))
        Kf = (lambda x, axes=(-2, -1): torch.fft.rfft2(x.float(), dim=axes))(Kp[:, None, :], axes=(-2, -1))
        Uf = (lambda x, axes=(-2, -1): torch.fft.rfft2(x.float(), dim=axes))(up[:, :, None, :], axes=(-2, -1))
        Yf = Uf * Kf[None, :, :, :]
        y = (lambda x, s, axes=(-2, -1): torch.fft.irfft2(x, s=s, dim=axes).to(torch.bfloat16))(Yf, (1, L2), axes=(-2, -1))[:, :, 0, :L]
        y = (lambda x, *d: x.permute(*d))(y, 0, 2, 1) + u * Dskip[None, None, :]
        return y

    def _B_ref(x, w_in, w_sp, b_sp, w_out):
        B, S, D = x.shape
        F = w_in.shape[-1]
        Fh = F // 2
        z = _gelu(torch.einsum('bsd,df->bsf', x, w_in))
        z1 = z[..., :Fh].clone()
        z2 = _layernorm(z[..., Fh:], axis=-1)
        g = torch.einsum('ts,bsf->btf', w_sp, z2) + b_sp[None, :, None]
        return torch.einsum('bsf,fd->bsd', z1 * g, w_out)
    x = A_u
    h = x + _A_ref(_layernorm(x, axis=-1), A_A_re, A_A_im, A_B_re, A_B_im, A_C_re, A_C_im, A_log_dt, A_Dskip)
    h2 = h + _B_ref(_layernorm(h, axis=-1), B_w_in, B_w_sp, B_b_sp, B_w_out)
    return h2
```

```cpp
#include <hip/hip_runtime.h>
#include <hip/hip_bf16.h>

typedef unsigned short u16;
typedef short bf16x8 __attribute__((ext_vector_type(8)));
typedef short s16x4 __attribute__((ext_vector_type(4)));
typedef float f32x4 __attribute__((ext_vector_type(4)));
typedef float f32x2 __attribute__((ext_vector_type(2)));
typedef unsigned u32x2 __attribute__((ext_vector_type(2)));
typedef unsigned u32x4 __attribute__((ext_vector_type(4)));
typedef __bf16 bf2_t __attribute__((ext_vector_type(2)));
#define LAS __attribute__((address_space(3)))
#define GAS __attribute__((address_space(1)))
typedef GAS unsigned gu32;
typedef GAS unsigned long long gu64;
#define RLX_AGENT __ATOMIC_RELAXED, __HIP_MEMORY_SCOPE_AGENT

constexpr int L_ = 2048, D_ = 576, N_ = 128, FH = 2304, F_ = 4608;

constexpr size_t WS_SLOT = 0;
constexpr size_t WS_FLAG = WS_SLOT + 262144;
constexpr size_t WS_UP = WS_FLAG + 65536;
constexpr size_t WS_XT = WS_UP + (size_t)D_ * L_ * 2;
constexpr size_t WS_HT = WS_XT + (size_t)D_ * L_ * 2;
constexpr size_t WS_H = WS_HT + (size_t)D_ * L_ * 2;
constexpr size_t WS_P = WS_H + (size_t)D_ * L_ * 2;
constexpr size_t WS_W1T = WS_P + (size_t)D_ * L_ * 4;
constexpr size_t WS_W3T = WS_W1T + (size_t)D_ * F_ * 2;
constexpr size_t WS_CSP = WS_W3T + (size_t)FH * D_ * 2;
constexpr size_t WS_CS = WS_CSP + 9 * F_ * 4;
constexpr size_t WS_Z1 = WS_CS + F_ * 4 + 1024 - (WS_CS + F_ * 4) % 1024;
constexpr size_t WS_Z2T = WS_Z1 + (size_t)L_ * FH * 2;
constexpr size_t WS_GZ = WS_Z2T + (size_t)L_ * FH * 2;
constexpr size_t WS_PART = WS_GZ + (size_t)L_ * FH * 2;

#define WAITV(n) asm volatile("s_waitcnt vmcnt(%0)" ::"n"(n) : "memory")
#define WAITL(n) asm volatile("s_waitcnt lgkmcnt(%0)" ::"n"(n) : "memory")
#define BAR() __builtin_amdgcn_s_barrier()
#define SCHED() __builtin_amdgcn_sched_barrier(0)

__device__ __forceinline__ unsigned cvtpk(float lo, float hi) {
  unsigned r; asm("v_cvt_pk_bf16_f32 %0, %1, %2" : "=v"(r) : "v"(lo), "v"(hi)); return r;
}
__device__ __forceinline__ float bflo(unsigned v) { return __uint_as_float(v << 16); }
__device__ __forceinline__ float bfhi(unsigned v) { return __uint_as_float(v & 0xffff0000u); }
__device__ __forceinline__ float bfr(float x) { return bflo(cvtpk(x, x)); }
__device__ __forceinline__ float bf2f(u16 v) { return __uint_as_float((unsigned)v << 16); }
__device__ __forceinline__ s16x4 trread(LAS const char* p) {
  return __builtin_amdgcn_ds_read_tr16_b64_v4i16((LAS s16x4*)p);
}
__device__ __forceinline__ float gelu_f(float x) {
  const float t = x * x;
  const float a = x * (t * (-2.3022082f * 0.044715f) + (-2.3022082f));
  return x * __builtin_amdgcn_rcpf(1.0f + __builtin_amdgcn_exp2f(a));
}
__device__ __forceinline__ f32x2 ln_scalars(float mean, float var) {
  const float mb = bfr(mean), vb = bfr(fmaxf(var, 0.f));
  const float sb = bfr(__builtin_sqrtf(bfr(vb + 1e-5f)));
  return (f32x2){mb, 1.0f / sb};
}

constexpr int K1_LN = 64, K1_KG = D_, K1_W1 = 288, K1_W3 = 144;
constexpr int K1_GRID = K1_LN + K1_KG + K1_W1 + K1_W3;
constexpr int K1_LDS = 2 * 32 * 1184;

struct PrepArgs {
  const u16 *x, *are, *aim, *bre, *bim, *cre, *cim, *logdt, *win, *wout;
  char* ws;
};

__device__ __forceinline__ void prep_ln1(const PrepArgs& a, int blk, LAS char* sm) {
  const int tid = threadIdx.x, lane = tid & 63, wid = tid >> 6;
  const int l0 = blk * 32, rl = wid * 8 + (lane >> 3), c8 = lane & 7;
  const u32x4* src = (const u32x4*)(a.x + (size_t)(l0 + rl) * D_);
  u32x4 v[9];
#pragma unroll
  for (int i = 0; i < 9; ++i) v[i] = src[c8 + 8 * i];
  float s = 0.f;
#pragma unroll
  for (int i = 0; i < 9; ++i)
#pragma unroll
    for (int j = 0; j < 4; ++j) s += bflo(v[i][j]) + bfhi(v[i][j]);
  s += __shfl_xor(s, 1); s += __shfl_xor(s, 2); s += __shfl_xor(s, 4);
  const float mean = s * (1.0f / D_);
  float q = 0.f;
#pragma unroll
  for (int i = 0; i < 9; ++i)
#pragma unroll
    for (int j = 0; j < 4; ++j) { const float d0 = bflo(v[i][j]) - mean, d1 = bfhi(v[i][j]) - mean; q += d0 * d0 + d1 * d1; }
  q += __shfl_xor(q, 1); q += __shfl_xor(q, 2); q += __shfl_xor(q, 4);
  const f32x2 sc = ln_scalars(mean, q * (1.0f / D_));
  LAS char* sx = sm; LAS char* su = sm + 32 * 1184;
#pragma unroll
  for (int i = 0; i < 9; ++i) {
    u32x4 u;
#pragma unroll
    for (int j = 0; j < 4; ++j) {
      const unsigned dd = cvtpk(bflo(v[i][j]) - sc.x, bfhi(v[i][j]) - sc.x);
      u[j] = cvtpk(bflo(dd) * sc.y, bfhi(dd) * sc.y);
    }
    *(LAS u32x4*)(sx + rl * 1184 + (c8 + 8 * i) * 16) = v[i];
    *(LAS u32x4*)(su + rl * 1184 + (c8 + 8 * i) * 16) = u;
  }
  __syncthreads();
  const int g = lane >> 4, qq = (lane & 15) >> 2, p = lane & 3;
  const int rd = (8 * g + qq) * 1184 + p * 8;
  char* xt = a.ws + WS_XT; char* up = a.ws + WS_UP;
#pragma unroll
  for (int i = 0; i < 9; ++i) {
    const int dg = wid * 9 + i, d = dg * 16 + (lane & 15);
    const s16x4 x0 = trread(sx + rd + dg * 32), x1 = trread(sx + rd + dg * 32 + 4 * 1184);
    const s16x4 u0 = trread(su + rd + dg * 32), u1 = trread(su + rd + dg * 32 + 4 * 1184);
    *(bf16x8*)(xt + (size_t)d * 4096 + (l0 + 8 * g) * 2) = __builtin_shufflevector(x0, x1, 0, 1, 2, 3, 4, 5, 6, 7);
    *(bf16x8*)(up + (size_t)d * 4096 + (g & 1) * 2048 + (l0 / 16 + (g >> 1)) * 16) = __builtin_shufflevector(u0, u1, 0, 1, 2, 3, 4, 5, 6, 7);
  }
  {
    u32x4* sl = (u32x4*)(a.ws + WS_SLOT);
    const u32x4 ones = {~0u, ~0u, ~0u, ~0u};
    sl[blk * 256 + tid] = ones;
    if (tid < 64) ((u32x4*)(a.ws + WS_FLAG))[blk * 64 + tid] = (u32x4){0u, 0u, 0u, 0u};
  }
}

__device__ __forceinline__ int kg_l0(int T, int r) {
  if (T == 0) return 128 * (r >> 3) + (r & 7);
  if (T == 1) return r < 8 ? 256 + 128 * (r >> 2) + 2 * (r & 3) : 512 + 128 * ((r - 8) >> 1) + 4 * (r & 1);
  return r <= 8 ? 1024 + 128 * r : 0;
}
__device__ __forceinline__ void cexp_rev(float zr2, float zir, float l, float& re, float& im) {
  const float m = __builtin_amdgcn_exp2f(zr2 * l);
  const float ph = __builtin_amdgcn_fractf(zir * l);
  re = m * __builtin_amdgcn_cosf(ph); im = m * __builtin_amdgcn_sinf(ph);
}
__device__ __forceinline__ void split2(float a, float b, unsigned& hi, unsigned& lo) {
  hi = cvtpk(a, b); lo = cvtpk(a - bflo(hi), b - bfhi(hi));
}

__device__ __forceinline__ void prep_kgen(const PrepArgs& a, int d, LAS char* sm) {
  const int tid = threadIdx.x, lane = tid & 63, wid = tid >> 6;
  LAS f32x4* prm = (LAS f32x4*)sm;
  LAS float* part = (LAS float*)(sm + 2048);
  LAS u16* ktab = (LAS u16*)(sm + 2048 + 12288);
  if (tid < N_) {
    const int i = d * N_ + tid;
    const float dt = bfr(__expf(bf2f(a.logdt[d])));
    const float zr = dt * bf2f(a.are[i]), zi = dt * bf2f(a.aim[i]);
    const float br = dt * bf2f(a.bre[i]), bi = dt * bf2f(a.bim[i]);
    const float cr = bf2f(a.cre[i]), ci = bf2f(a.cim[i]);
    prm[tid] = (f32x4){zr * 1.44269504f, zi * 0.15915494f, cr * br - ci * bi, cr * bi + ci * br};
  }
  __syncthreads();
  const int r = lane & 15, kg = lane >> 4;
  const float l0f[3] = {(float)kg_l0(0, r), (float)kg_l0(1, r), (float)kg_l0(2, r)};
  const float lb = (float)(8 * r);
  f32x4 acc[3] = {};
#pragma unroll
  for (int kk = 0; kk < 2; ++kk) {
    u32x4 ah[3], al[3], bh, bl;
#pragma unroll
    for (int i = 0; i < 4; ++i) {
      const f32x4 pp = prm[(wid * 2 + kk) * 16 + kg * 4 + i];
      float re, im;
#pragma unroll
      for (int T = 0; T < 3; ++T) { cexp_rev(pp[0], pp[1], l0f[T], re, im); unsigned h, l; split2(re, im, h, l); ah[T][i] = h; al[T][i] = l; }
      cexp_rev(pp[0], pp[1], lb, re, im);
      const float fr = re * pp[2] - im * pp[3], fi = re * pp[3] + im * pp[2];
      unsigned h, l; split2(fr, -fi, h, l); bh[i] = h; bl[i] = l;
    }
#pragma unroll
    for (int T = 0; T < 3; ++T) {
      acc[T] = __builtin_amdgcn_mfma_f32_16x16x32_bf16(__builtin_bit_cast(bf16x8, al[T]), __builtin_bit_cast(bf16x8, bh), acc[T], 0, 0, 0);
      acc[T] = __builtin_amdgcn_mfma_f32_16x16x32_bf16(__builtin_bit_cast(bf16x8, ah[T]), __builtin_bit_cast(bf16x8, bl), acc[T], 0, 0, 0);
      acc[T] = __builtin_amdgcn_mfma_f32_16x16x32_bf16(__builtin_bit_cast(bf16x8, ah[T]), __builtin_bit_cast(bf16x8, bh), acc[T], 0, 0, 0);
    }
  }
#pragma unroll
  for (int T = 0; T < 3; ++T)
#pragma unroll
    for (int j = 0; j < 4; ++j) part[wid * 768 + T * 256 + (kg * 4 + j) * 16 + r] = acc[T][j];
  __syncthreads();
#pragma unroll
  for (int T = 0; T < 3; ++T) {
    const int e = T * 256 + tid, row = tid >> 4, b = tid & 15;
    const float k = (part[e] + part[768 + e]) + (part[1536 + e] + part[2304 + e]);
    const int l = kg_l0(T, row) + 8 * b;
    if (T < 2 || row < 8 || (row == 8 && b == 0)) ktab[l] = (u16)(cvtpk(k, k) & 0xffffu);
  }
  __syncthreads();
  unsigned kv[9];
#pragma unroll
  for (int i = 0; i < 9; ++i) {
    const int T = tid * 8 - 1 + i;
    const int rho = (int)bfr((float)(T < 0 ? 0 : T));
    kv[i] = T < 0 ? 0u : (unsigned)ktab[rho];
  }
  u32x4 o0, o1;
#pragma unroll
  for (int i = 0; i < 4; ++i) { o0[i] = kv[i + 1] | (kv[i] << 16); o1[i] = kv[i + 5] | (kv[i + 4] << 16); }
  u32x4* dst = (u32x4*)(a.ws + WS_P + (size_t)d * 8192 + tid * 32);
  dst[0] = o0; dst[1] = o1;
}

__device__ __forceinline__ void prep_wt(const u16* src, int ld, char* dst, float* csum, LAS char* sm) {
  const int tid = threadIdx.x, lane = tid & 63, wid = tid >> 6;
  u32x4 v[5];
#pragma unroll
  for (int i = 0; i < 5; ++i) {
    const int id = i * 256 + tid, row = id / 18, c = id - row * 18;
    if (id < 1152) v[i] = *(const u32x4*)(src + (size_t)row * ld + c * 8);
  }
#pragma unroll
  for (int i = 0; i < 5; ++i) { const int id = i * 256 + tid; if (id < 1152) *(LAS u32x4*)(sm + id * 16) = v[i]; }
  __syncthreads();
  const int g = lane >> 4, q = (lane & 15) >> 2, p = lane & 3;
  for (int it = wid; it < 18; it += 4) {
    const int nt = it >> 1, ch = (it & 1) * 4 + g, n = nt * 16 + (lane & 15);
    LAS const char* rp = sm + (8 * ch + q) * 288 + nt * 32 + p * 8;
    const s16x4 a0 = trread(rp), a1 = trread(rp + 4 * 288);
    *(bf16x8*)(dst + n * 128 + ((ch ^ (n & 7)) << 4)) = __builtin_shufflevector(a0, a1, 0, 1, 2, 3, 4, 5, 6, 7);
  }
  if (csum && tid < 72) {
    float s0 = 0.f, s1 = 0.f;
#pragma unroll 8
    for (int k = 0; k < 64; ++k) { const unsigned w = *(LAS const unsigned*)(sm + k * 288 + tid * 4); s0 += bflo(w); s1 += bfhi(w); }
    *(f32x2*)(csum + tid * 2) = (f32x2){s0, s1};
  }
}

__global__ __launch_bounds__(256) void k1_prep(PrepArgs a) {
  extern __shared__ __attribute__((aligned(16))) char smem[];
  LAS char* sm = (LAS char*)smem;
  int b = blockIdx.x;
  if (b < K1_LN) { prep_ln1(a, b, sm); return; }
  b -= K1_LN;
  if (b < K1_KG) { prep_kgen(a, b, sm); return; }
  b -= K1_KG;
  if (b < K1_W1) {
    const int tn = b / 9, kt = b - tn * 9;
    prep_wt(a.win + (size_t)kt * 64 * F_ + tn * 144, F_, a.ws + WS_W1T + (size_t)b * 18432, (float*)(a.ws + WS_CSP) + kt * F_ + tn * 144, sm);
    return;
  }
  b -= K1_W1;
  {
    const int tn = b / 36, kt = b - tn * 36;
    prep_wt(a.wout + (size_t)kt * 64 * D_ + tn * 144, D_, a.ws + WS_W3T + (size_t)b * 18432, nullptr, sm);
  }
}

constexpr int K2_CS = 72, K2_PF = 256;
constexpr int K2_GRID = D_ + K2_CS + K2_PF;
constexpr int CV_P = 64, CV_U0 = CV_P + 8192 + 256, CV_U1 = CV_U0 + 2048 + 256, K2_LDS = CV_U1 + 2048;

__global__ __launch_bounds__(64) void k2_conv(char* ws, const u16* dskip, const u16* wsp) {
  extern __shared__ __attribute__((aligned(16))) char smem[];
  LAS char* sm = (LAS char*)smem;
  const int lane = threadIdx.x;
  int d = blockIdx.x;
  if (d >= D_) {
    d -= D_;
    if (d < K2_CS) {
      const float* csp = (const float*)(ws + WS_CSP); const int f = d * 64 + lane; float s = 0.f;
#pragma unroll
      for (int k = 0; k < 9; ++k) s += csp[k * F_ + f];
      ((float*)(ws + WS_CS))[f] = s;
    } else {
      d -= K2_CS;
      auto rs = __builtin_amdgcn_make_buffer_rsrc((void*)wsp, 0, L_ * L_ * 2, 0x00020000);
#pragma unroll
      for (int i = 0; i < 32; ++i) __builtin_amdgcn_raw_ptr_buffer_load_lds(rs, (LAS void*)(sm + (i & 7) * 1024), 16, lane * 16, d * 32768 + i * 1024, 0, 0);
      WAITV(0);
    }
    return;
  }
  auto rp = __builtin_amdgcn_make_buffer_rsrc((void*)(ws + WS_P + (size_t)d * 8192), 0, 8192, 0x00020000);
  auto ru = __builtin_amdgcn_make_buffer_rsrc((void*)(ws + WS_UP + (size_t)d * 4096), 0, 4096, 0x00020000);
#pragma unroll
  for (int i = 0; i < 2; ++i) {
    __builtin_amdgcn_raw_ptr_buffer_load_lds(ru, (LAS void*)(sm + CV_U0 + i * 1024), 16, lane * 16, i * 1024, 0, 0);
    __builtin_amdgcn_raw_ptr_buffer_load_lds(ru, (LAS void*)(sm + CV_U1 + i * 1024), 16, lane * 16, 2048 + i * 1024, 0, 0);
  }
#pragma unroll
  for (int i = 0; i < 8; ++i) __builtin_amdgcn_raw_ptr_buffer_load_lds(rp, (LAS void*)(sm + CV_P + i * 1024), 16, lane * 16, i * 1024, 0, 0);
  const int n = lane & 15, kg = lane >> 4;
  u32x2 xv[8];
  const char* xt = ws + WS_XT + (size_t)d * 4096 + n * 32 + kg * 8;
#pragma unroll
  for (int t = 0; t < 8; ++t) xv[t] = *(const u32x2*)(xt + t * 512);
  const float dsk = bf2f(dskip[d]);
  if (lane < 16) *(LAS unsigned*)(sm + lane * 4) = 0u;
  *(LAS unsigned*)(sm + CV_U0 - 256 + lane * 4) = 0u;
  *(LAS unsigned*)(sm + CV_U1 - 256 + lane * 4) = 0u;
  WAITV(0); WAITL(0);
  LAS const char* pa = sm + CV_P + 4 * (n + 16 * (kg & 1) - 8 * (kg >> 1) - 6);
  LAS const char* pb = sm + ((kg >> 1) ? CV_U1 : CV_U0) + 16 * (n - (kg & 1));
  f32x4 acc[8] = {};
#pragma unroll
  for (int r = 0; r < 8; ++r) {
    bf16x8 A[8], B[8];
#pragma unroll
    for (int c = 0; c < 8; ++c) {
      LAS const unsigned* q = (LAS const unsigned*)(pa + (8 * c + r) * 128);
      A[c] = __builtin_bit_cast(bf16x8, (u32x4){q[6], q[4], q[2], q[0]});
      B[c] = *(LAS const bf16x8*)(pb + (8 * c - r) * 32);
    }
#pragma unroll
    for (int c = 0; c < 8; ++c)
#pragma unroll
      for (int e = 0; e + c < 8; ++e) acc[c + e] = __builtin_amdgcn_mfma_f32_16x16x32_bf16(A[c], B[e], acc[c + e], 0, 0, 0);
  }
  char* ht = ws + WS_HT + (size_t)d * 4096 + n * 32 + kg * 8;
  LAS const char* pu = sm + ((kg >> 1) ? CV_U1 : CV_U0) + 16 * n + 8 * (kg & 1);
#pragma unroll
  for (int t = 0; t < 8; ++t) {
    const u32x2 uv = *(LAS const u32x2*)(pu + t * 256);
    u32x2 o;
#pragma unroll
    for (int j = 0; j < 2; ++j) {
      const unsigned y = cvtpk(acc[t][2 * j], acc[t][2 * j + 1]);
      const unsigned sk = cvtpk(bflo(uv[j]) * dsk, bfhi(uv[j]) * dsk);
      const unsigned s = cvtpk(bflo(y) + bflo(sk), bfhi(y) + bfhi(sk));
      o[j] = cvtpk(bflo(xv[t][j]) + bflo(s), bfhi(xv[t][j]) + bfhi(s));
    }
    *(u32x2*)(ht + t * 512) = o;
  }
}

constexpr int STG = 272 * 128;
constexpr int RING = 4 * STG;
constexpr int GEMM_LDS = RING + 4096;

__device__ __forceinline__ void tile_of(int b, int& tm, int& tn) {
  const int xcd = b & 7, idx = b >> 3;
  tm = (xcd & 1) * 8 + (idx & 7); tn = (xcd >> 1) * 4 + (idx >> 3);
}
__device__ __forceinline__ void issue_stage(LAS char* sm, int slot, int w, __amdgpu_buffer_rsrc_t ra, unsigned va, int sa, int pa,
                                            __amdgpu_buffer_rsrc_t rb, unsigned vb, int sb) {
#pragma unroll
  for (int i = 0; i < 4; ++i)
    __builtin_amdgcn_raw_ptr_buffer_load_lds(ra, (LAS void*)(sm + slot * STG + (4 * i + w) * 1024), 16, va, sa + (4 * i + w) * pa, 0, 0);
#pragma unroll
  for (int i = 0; i < 4; ++i)
    __builtin_amdgcn_raw_ptr_buffer_load_lds(rb, (LAS void*)(sm + slot * STG + 16384 + (4 * i + w) * 1024), 16, vb, sb + (4 * i + w) * 1024, 0, 0);
  if (w < 2) __builtin_amdgcn_raw_ptr_buffer_load_lds(rb, (LAS void*)(sm + slot * STG + 16384 + (16 + w) * 1024), 16, vb, sb + (16 + w) * 1024, 0, 0);
}
template <int S> __device__ __forceinline__ void wait_stages(int w) { if (w < 2) WAITV(S * 9); else WAITV(S * 8); }

#define MM(F) do { _Pragma("unroll") for (int nt = 0; nt < 9; ++nt) _Pragma("unroll") for (int mt = 0; mt < 2; ++mt) \
    acc[mt][nt] = __builtin_amdgcn_mfma_f32_16x16x32_bf16(F[2 + nt], F[mt], acc[mt][nt], 0, 0, 0); } while (0)
#define RDB(F, so, ks) do { _Pragma("unroll") for (int nt = 0; nt < 9; ++nt) \
    F[2 + nt] = *(const LAS bf16x8*)(sm + (so) + (boff ^ ((ks) * 64)) + nt * 2048); } while (0)
#define RDA(F, so, ks) do { _Pragma("unroll") for (int mt = 0; mt < 2; ++mt) \
    F[mt] = *(const LAS bf16x8*)(sm + (so) + (aoff ^ ((ks) * 64)) + mt * 2048); } while (0)
#define RDAT(F, so, ks) do { _Pragma("unroll") for (int mt = 0; mt < 2; ++mt) { \
    const s16x4 t0_ = trread(sm + (so) + (aoff ^ (mt * 32)) + (ks) * 8192), t1_ = trread(sm + (so) + (aoff ^ (mt * 32)) + (ks) * 8192 + 1024); \
    F[mt] = __builtin_shufflevector(t0_, t1_, 0, 1, 2, 3, 4, 5, 6, 7); } } while (0)
#define ILV(NR) do { _Pragma("unroll") for (int q_ = 0; q_ < (NR); ++q_) { __builtin_amdgcn_sched_group_barrier(0x100, 1, 0); __builtin_amdgcn_sched_group_barrier(0x8, 1, 0); } \
    __builtin_amdgcn_sched_group_barrier(0x8, 18 - (NR), 0); } while (0)
#define ZERO_ACC() do { _Pragma("unroll") for (int mt = 0; mt < 2; ++mt) _Pragma("unroll") for (int nt = 0; nt < 9; ++nt) acc[mt][nt] = (f32x4){0.f, 0.f, 0.f, 0.f}; } while (0)

__global__ __launch_bounds__(512, 2) void k3_gemm1(char* ws) {
  extern __shared__ __attribute__((aligned(16))) char smem[];
  LAS char* sm = (LAS char*)smem;
  const int lane = threadIdx.x & 63, wid = __builtin_amdgcn_readfirstlane(threadIdx.x >> 6);
  int tm, tn; tile_of(blockIdx.x, tm, tn);
  if (wid >= 4) {
    const int w = wid - 4;
    auto ra = __builtin_amdgcn_make_buffer_rsrc((void*)(ws + WS_HT), 0, D_ * L_ * 2, 0x00020000);
    auto rb = __builtin_amdgcn_make_buffer_rsrc((void*)(ws + WS_W1T), 0, D_ * F_ * 2, 0x00020000);
    const unsigned f = (lane >> 4) | ((w >> 1) << 2);
    const unsigned va = (lane >> 4) * 4096 + ((((lane & 15) >> 1) ^ f) << 5) + (lane & 1) * 16, vb = lane * 16;
#define ISS(s) issue_stage(sm, (s) & 3, w, ra, va, ((s) < 9 ? (s) : (s) - 9) * 64 * 4096 + tm * 256, 4 * 4096, rb, vb, ((s) < 9 ? (16 + tn) * 9 + (s) : tn * 9 + (s) - 9) * 18432)
    ISS(0); ISS(1); ISS(2); ISS(3);
    wait_stages<3>(w); BAR();
    for (int s = 0; s < 14; ++s) { wait_stages<2>(w); BAR(); ISS(s + 4); }
    wait_stages<2>(w); BAR(); wait_stages<1>(w); BAR(); wait_stages<0>(w); BAR();
#undef ISS
    return;
  }
  const int fr = lane & 15, kg = lane >> 4;
  const int aoff = (8 * kg + (fr >> 2)) * 256 + (((2 * wid) ^ ((fr >> 2) | ((kg & 1) << 2))) << 5) + (lane & 3) * 8;
  const int boff = 16384 + fr * 128 + ((kg ^ (lane & 7)) << 4);
  const float* cs = (const float*)(ws + WS_CS);
  f32x4 acc[2][9];
  bf16x8 F0[11], F1[11];
  float sum[2] = {0.f, 0.f}, sq[2] = {0.f, 0.f};
  char* hrow = ws + WS_H + (size_t)(tm * 128 + wid * 32 + fr) * (D_ * 2) + kg * 16;
#define STATS(F, kidx) do { _Pragma("unroll") for (int mt = 0; mt < 2; ++mt) { const u32x4 w_ = __builtin_bit_cast(u32x4, F[mt]); \
      _Pragma("unroll") for (int j = 0; j < 4; ++j) { const unsigned e_ = w_[j]; const bf2_t h_ = __builtin_bit_cast(bf2_t, e_); \
        sum[mt] = __builtin_amdgcn_fdot2_f32_bf16(h_, __builtin_bit_cast(bf2_t, 0x3f803f80u), sum[mt], false); \
        sq[mt] = __builtin_amdgcn_fdot2_f32_bf16(h_, h_, sq[mt], false); } \
      asm volatile("" : "+v"(sum[mt]), "+v"(sq[mt])); \
      if (((kidx) & 15) == tn) *(bf16x8*)(hrow + mt * 16 * (D_ * 2) + (kidx) * 64) = F[mt]; } } while (0)
#define ITER(s, DOSTAT, LAST) do { \
    RDAT(F1, ((s) & 3) * STG, 1); RDB(F1, ((s) & 3) * STG, 1); MM(F0); ILV(13); SCHED(); \
    if (DOSTAT) STATS(F0, 2 * (s)); \
    if ((s) != 17) { WAITL(0); BAR(); SCHED(); } \
    if (!(LAST)) { RDAT(F0, (((s) + 1) & 3) * STG, 0); RDB(F0, (((s) + 1) & 3) * STG, 0); } \
    MM(F1); if (!(LAST)) ILV(13); SCHED(); \
    if (DOSTAT) STATS(F1, 2 * (s) + 1); } while (0)
  ZERO_ACC();
  BAR();
  RDAT(F0, 0, 0); RDB(F0, 0, 0);
  ITER(0, true, false); ITER(1, true, false); ITER(2, true, false); ITER(3, true, false); ITER(4, true, false);
  ITER(5, true, false); ITER(6, true, false); ITER(7, true, false); ITER(8, true, true);
  float mu[2], rs[2];
#pragma unroll
  for (int mt = 0; mt < 2; ++mt) {
    float s = sum[mt], q = sq[mt];
    s += __shfl_xor(s, 16); s += __shfl_xor(s, 32); q += __shfl_xor(q, 16); q += __shfl_xor(q, 32);
    const float mean = s * (1.0f / D_);
    const f32x2 sc = ln_scalars(mean, q * (1.0f / D_) - mean * mean);
    mu[mt] = sc.x; rs[mt] = sc.y;
  }
  u32x2 z2[2][9];
  {
    gu64* slot = (gu64*)(ws + WS_SLOT) + ((tm * 4 + wid) * 16 + tn) * 32;
#pragma unroll
    for (int mt = 0; mt < 2; ++mt) {
      float s = 0.f;
#pragma unroll
      for (int nt = 0; nt < 9; ++nt) {
        const f32x4 c4 = *(const f32x4*)(cs + FH + tn * 144 + nt * 16 + kg * 4);
        float v[4];
#pragma unroll
        for (int j = 0; j < 4; ++j) v[j] = gelu_f((acc[mt][nt][j] - mu[mt] * c4[j]) * rs[mt]);
        z2[mt][nt] = (u32x2){cvtpk(v[0], v[1]), cvtpk(v[2], v[3])};
        s += (bflo(z2[mt][nt][0]) + bfhi(z2[mt][nt][0])) + (bflo(z2[mt][nt][1]) + bfhi(z2[mt][nt][1]));
      }
      s += __shfl_xor(s, 16); s += __shfl_xor(s, 32);
      const float mt_mean = s * (1.0f / 144.0f);
      float q = 0.f;
#pragma unroll
      for (int nt = 0; nt < 9; ++nt)
#pragma unroll
        for (int j = 0; j < 2; ++j) { const float d0 = bflo(z2[mt][nt][j]) - mt_mean, d1 = bfhi(z2[mt][nt][j]) - mt_mean; q += d0 * d0 + d1 * d1; }
      q += __shfl_xor(q, 16); q += __shfl_xor(q, 32);
      unsigned qb = __float_as_uint(q); if (qb == 0xffffffffu) qb = 0x7fc00000u;
      if (kg == 0) __hip_atomic_store(slot + mt * 16 + fr, ((unsigned long long)qb << 32) | __float_as_uint(mt_mean), RLX_AGENT);
    }
  }
#pragma unroll
  for (int mt = 0; mt < 2; ++mt)
#pragma unroll
    for (int nt = 0; nt < 9; ++nt) asm volatile("" : "+v"(z2[mt][nt]));
  ZERO_ACC();
  RDAT(F0, (9 & 3) * STG, 0); RDB(F0, (9 & 3) * STG, 0);
  ITER(9, false, false); ITER(10, false, false); ITER(11, false, false); ITER(12, false, false); ITER(13, false, false);
  ITER(14, false, false); ITER(15, false, false); ITER(16, false, false); ITER(17, false, true);
#undef ITER
#undef STATS
  {
    u32x4* z1 = (u32x4*)(ws + WS_Z1) + ((size_t)((tm * 16 + tn) * 4 + wid) * 9) * 64 + lane;
#pragma unroll
    for (int nt = 0; nt < 9; ++nt) {
      const f32x4 c4 = *(const f32x4*)(cs + tn * 144 + nt * 16 + kg * 4);
      u32x4 o;
#pragma unroll
      for (int mt = 0; mt < 2; ++mt) {
        float v[4];
#pragma unroll
        for (int j = 0; j < 4; ++j) v[j] = gelu_f((acc[mt][nt][j] - mu[mt] * c4[j]) * rs[mt]);
        o[2 * mt] = cvtpk(v[0], v[1]); o[2 * mt + 1] = cvtpk(v[2], v[3]);
      }
      z1[nt * 64] = o;
    }
  }
  float m3[2], r3[2];
  {
    gu64* slot = (gu64*)(ws + WS_SLOT) + (tm * 4 + wid) * 512 + lane;
    unsigned long long g[8];
    for (unsigned spins = 0; spins < (1u << 20); ++spins) {
      bool ok = true;
#pragma unroll
      for (int i = 0; i < 8; ++i) { g[i] = __hip_atomic_load(slot + i * 64, RLX_AGENT); ok &= (unsigned)(g[i] >> 32) != 0xffffffffu; }
      if (__all(ok)) break;
      __builtin_amdgcn_s_sleep(2);
    }
    float s = 0.f;
#pragma unroll
    for (int i = 0; i < 8; ++i) s += __uint_as_float((unsigned)g[i]);
    s += __shfl_xor(s, 32);
    const float mean = s * (1.0f / 16.0f);
    float q = 0.f;
#pragma unroll
    for (int i = 0; i < 8; ++i) { const float dm = __uint_as_float((unsigned)g[i]) - mean; q += __uint_as_float((unsigned)(g[i] >> 32)) + 144.0f * dm * dm; }
    q += __shfl_xor(q, 32);
    const f32x2 sc = ln_scalars(mean, q * (1.0f / FH));
#pragma unroll
    for (int mt = 0; mt < 2; ++mt) { m3[mt] = __shfl(sc.x, mt * 16 + fr); r3[mt] = __shfl(sc.y, mt * 16 + fr); }
  }
  {
    LAS char* tb = sm + 2 * STG + wid * (32 * 296);
#pragma unroll
    for (int mt = 0; mt < 2; ++mt)
#pragma unroll
      for (int nt = 0; nt < 9; ++nt) {
        u32x2 o;
#pragma unroll
        for (int j = 0; j < 2; ++j) {
          const unsigned dd = cvtpk(bflo(z2[mt][nt][j]) - m3[mt], bfhi(z2[mt][nt][j]) - m3[mt]);
          o[j] = cvtpk(bflo(dd) * r3[mt], bfhi(dd) * r3[mt]);
        }
        *(LAS u32x2*)(tb + (mt * 16 + fr) * 296 + nt * 32 + kg * 8) = o;
      }
    WAITL(0);
    char* dst = ws + WS_Z2T + (size_t)(tn * 32 + 2 * tm + (wid >> 1)) * 18432;
    const int ch = (wid & 1) * 4 + kg;
    LAS const char* rp = tb + (8 * kg + (fr >> 2)) * 296 + (lane & 3) * 8;
#pragma unroll
    for (int nt = 0; nt < 9; ++nt) {
      const s16x4 a0 = trread(rp + nt * 32), a1 = trread(rp + nt * 32 + 4 * 296);
      *(bf16x8*)(dst + (nt * 16 + fr) * 128 + ((ch ^ (fr & 7)) << 4)) = __builtin_shufflevector(a0, a1, 0, 1, 2, 3, 4, 5, 6, 7);
    }
  }
}

__global__ __launch_bounds__(512, 2) void k4_gemm2(char* ws, const u16* wsp, const u16* bsp) {
  extern __shared__ __attribute__((aligned(16))) char smem[];
  LAS char* sm = (LAS char*)smem;
  const int lane = threadIdx.x & 63, wid = __builtin_amdgcn_readfirstlane(threadIdx.x >> 6);
  int tm, tn; tile_of(blockIdx.x, tm, tn);
  if (wid >= 4) {
    const int w = wid - 4;
    auto ra = __builtin_amdgcn_make_buffer_rsrc((void*)(wsp + (size_t)tm * 128 * L_), 0, 128 * L_ * 2, 0x00020000);
    auto rb = __builtin_amdgcn_make_buffer_rsrc((void*)(ws + WS_Z2T + (size_t)tn * 32 * 18432), 0, 32 * 18432, 0x00020000);
    const unsigned va = (lane >> 3) * 4096 + (((lane & 7) ^ (lane >> 3)) << 4), vb = lane * 16;
#define ISS(s) issue_stage(sm, (s) & 3, w, ra, va, (s) * 128, 8 * 4096, rb, vb, (s) * 18432)
    ISS(0); ISS(1); ISS(2); ISS(3);
    wait_stages<3>(w); BAR();
    for (int s = 0; s < 28; ++s) { wait_stages<2>(w); BAR(); ISS(s + 4); }
    wait_stages<2>(w); BAR(); wait_stages<1>(w); BAR(); wait_stages<0>(w); BAR();
#undef ISS
    return;
  }
  const int fr = lane & 15, kg = lane >> 4;
  const int aoff = (wid * 32 + fr) * 128 + ((kg ^ (lane & 7)) << 4);
  const int boff = 16384 + fr * 128 + ((kg ^ (lane & 7)) << 4);
  f32x4 acc[2][9];
  bf16x8 F0[11], F1[11];
  u32x4 z1[9];
  {
    const u32x4* zp = (const u32x4*)(ws + WS_Z1) + ((size_t)((tm * 16 + tn) * 4 + wid) * 9) * 64 + lane;
#pragma unroll
    for (int nt = 0; nt < 9; ++nt) z1[nt] = zp[nt * 64];
  }
  const float b0 = bf2f(bsp[tm * 128 + wid * 32 + fr]), b1 = bf2f(bsp[tm * 128 + wid * 32 + 16 + fr]);
#define ITER(slot, LAST) do { \
    RDA(F1, (slot) * STG, 1); RDB(F1, (slot) * STG, 1); MM(F0); ILV(11); SCHED(); \
    if (!(LAST)) { WAITL(0); BAR(); SCHED(); RDA(F0, (((slot) + 1) & 3) * STG, 0); RDB(F0, (((slot) + 1) & 3) * STG, 0); } \
    MM(F1); if (!(LAST)) ILV(11); SCHED(); } while (0)
  ZERO_ACC();
  BAR();
  RDA(F0, 0, 0); RDB(F0, 0, 0);
  for (int s = 0; s < 28; s += 4) { ITER(0, false); ITER(1, false); ITER(2, false); ITER(3, false); }
  ITER(0, false); ITER(1, false); ITER(2, false); ITER(3, true);
#undef ITER
  LAS char* tb = sm + wid * (32 * 304);
#pragma unroll
  for (int mt = 0; mt < 2; ++mt)
#pragma unroll
    for (int nt = 0; nt < 9; ++nt) {
      const float b = mt ? b1 : b0;
      u32x2 o;
#pragma unroll
      for (int j = 0; j < 2; ++j) {
        const unsigned z = z1[nt][2 * mt + j];
        o[j] = cvtpk(bflo(z) * (acc[mt][nt][2 * j] + b), bfhi(z) * (acc[mt][nt][2 * j + 1] + b));
      }
      *(LAS u32x2*)(tb + (mt * 16 + fr) * 304 + nt * 32 + kg * 8) = o;
    }
  WAITL(0);
  char* dst = ws + WS_GZ + (size_t)(tm * 128 + wid * 32) * (FH * 2) + tn * 288;
#pragma unroll
  for (int i = 0; i < 9; ++i) {
    const int id = i * 64 + lane, row = id / 18, c = id - row * 18;
    *(u32x4*)(dst + (size_t)row * (FH * 2) + c * 16) = *(LAS const u32x4*)(tb + row * 304 + c * 16);
  }
}

__global__ __launch_bounds__(512, 2) void k5_gemm3(char* ws, u16* out) {
  extern __shared__ __attribute__((aligned(16))) char smem[];
  LAS char* sm = (LAS char*)smem;
  const int lane = threadIdx.x & 63, wid = __builtin_amdgcn_readfirstlane(threadIdx.x >> 6);
  int tm, tn; tile_of(blockIdx.x, tm, tn);
  const int kq = tn >> 2, tc = tn & 3;
  if (wid >= 4) {
    const int w = wid - 4;
    auto ra = __builtin_amdgcn_make_buffer_rsrc((void*)(ws + WS_GZ + (size_t)tm * 128 * FH * 2), 0, 128 * FH * 2, 0x00020000);
    auto rb = __builtin_amdgcn_make_buffer_rsrc((void*)(ws + WS_W3T + (size_t)(tc * 36 + kq * 9) * 18432), 0, 9 * 18432, 0x00020000);
    const unsigned va = (lane >> 3) * (FH * 2) + (((lane & 7) ^ (lane >> 3)) << 4), vb = lane * 16;
#define ISS(s) issue_stage(sm, (s) & 3, w, ra, va, (kq * 9 + (s)) * 128, 8 * FH * 2, rb, vb, (s) * 18432)
    ISS(0); ISS(1); ISS(2); ISS(3);
    wait_stages<3>(w); BAR();
    for (int s = 0; s < 5; ++s) { wait_stages<2>(w); BAR(); ISS(s + 4); }
    wait_stages<2>(w); BAR(); wait_stages<1>(w); BAR(); wait_stages<0>(w); BAR();
#undef ISS
    return;
  }
  const int fr = lane & 15, kg = lane >> 4;
  const int aoff = (wid * 32 + fr) * 128 + ((kg ^ (lane & 7)) << 4);
  const int boff = 16384 + fr * 128 + ((kg ^ (lane & 7)) << 4);
  f32x4 acc[2][9];
  bf16x8 F0[11], F1[11];
  const size_t orow = (size_t)(tm * 128 + wid * 32 + fr) * D_ + tc * 144 + kg * 4;
  if (wid == kq) {
    const u16* hp = (const u16*)(ws + WS_H) + orow;
#pragma unroll
    for (int mt = 0; mt < 2; ++mt)
#pragma unroll
      for (int nt = 0; nt < 9; ++nt) {
        const u32x2 h = *(const u32x2*)(hp + mt * 16 * D_ + nt * 16);
        acc[mt][nt] = (f32x4){bflo(h[0]), bfhi(h[0]), bflo(h[1]), bfhi(h[1])};
      }
  } else ZERO_ACC();
#define ITER(s, LAST) do { \
    RDA(F1, ((s) & 3) * STG, 1); RDB(F1, ((s) & 3) * STG, 1); MM(F0); ILV(11); SCHED(); \
    if (!(LAST)) { WAITL(0); BAR(); SCHED(); RDA(F0, (((s) + 1) & 3) * STG, 0); RDB(F0, (((s) + 1) & 3) * STG, 0); } \
    MM(F1); if (!(LAST)) ILV(11); SCHED(); } while (0)
  BAR();
  RDA(F0, 0, 0); RDB(F0, 0, 0);
  ITER(0, false); ITER(1, false); ITER(2, false); ITER(3, false); ITER(4, false); ITER(5, false); ITER(6, false); ITER(7, false); ITER(8, true);
#undef ITER
  auto rp = __builtin_amdgcn_make_buffer_rsrc((void*)(ws + WS_PART + (size_t)(tm * 4 + tc) * 16 * 18432), 0, 16 * 18432, 0x00020000);
  gu32* flags = (gu32*)(ws + WS_FLAG) + (tm * 4 + tc) * 4 * 64;
  if (wid != kq) {
#pragma unroll
    for (int mt = 0; mt < 2; ++mt)
#pragma unroll
      for (int nt = 0; nt < 9; ++nt)
        __builtin_amdgcn_raw_buffer_store_b128(__builtin_bit_cast(u32x4, acc[mt][nt]), rp, lane * 16 + (mt * 9 + nt) * 1024, (wid * 4 + kq) * 18432, 16);
  }
  WAITV(0);
  BAR();
  if (wid != kq) return;
  if (lane == 0) __hip_atomic_store(flags + kq * 64, 1u, RLX_AGENT);
  for (unsigned spins = 0; spins < (1u << 20); ++spins) {
    const unsigned fv = __hip_atomic_load(flags + (lane & 3) * 64, RLX_AGENT);
    if (__all(fv == 1u)) break;
    __builtin_amdgcn_s_sleep(1);
  }
  __builtin_amdgcn_fence(__ATOMIC_ACQUIRE, "wavefront");
#pragma unroll
  for (int sidx = 1; sidx < 4; ++sidx) {
    const int src = (kq + sidx) & 3;
#pragma unroll
    for (int mt = 0; mt < 2; ++mt)
#pragma unroll
      for (int nt = 0; nt < 9; ++nt)
        acc[mt][nt] += __builtin_bit_cast(f32x4, __builtin_amdgcn_raw_buffer_load_b128(rp, lane * 16 + (mt * 9 + nt) * 1024, (kq * 4 + src) * 18432, 16));
  }
#pragma unroll
  for (int mt = 0; mt < 2; ++mt)
#pragma unroll
    for (int nt = 0; nt < 9; ++nt)
      *(u32x2*)(out + orow + mt * 16 * D_ + nt * 16) = (u32x2){cvtpk(acc[mt][nt][0], acc[mt][nt][1]), cvtpk(acc[mt][nt][2], acc[mt][nt][3])};
}

extern "C" void kernel_launch(void* const* d_in, const int* in_sizes, int n_in, void* d_out, int out_size, void* d_ws,
                              size_t ws_size, hipStream_t stream) {
  char* ws = (char*)d_ws;
  PrepArgs pa;
  pa.x = (const u16*)d_in[0]; pa.are = (const u16*)d_in[1]; pa.aim = (const u16*)d_in[2]; pa.bre = (const u16*)d_in[3];
  pa.bim = (const u16*)d_in[4]; pa.cre = (const u16*)d_in[5]; pa.cim = (const u16*)d_in[6]; pa.logdt = (const u16*)d_in[7];
  pa.win = (const u16*)d_in[10]; pa.wout = (const u16*)d_in[13]; pa.ws = ws;
  (void)hipFuncSetAttribute((const void*)k1_prep, hipFuncAttributeMaxDynamicSharedMemorySize, K1_LDS);
  (void)hipFuncSetAttribute((const void*)k3_gemm1, hipFuncAttributeMaxDynamicSharedMemorySize, GEMM_LDS);
  (void)hipFuncSetAttribute((const void*)k4_gemm2, hipFuncAttributeMaxDynamicSharedMemorySize, GEMM_LDS);
  (void)hipFuncSetAttribute((const void*)k5_gemm3, hipFuncAttributeMaxDynamicSharedMemorySize, GEMM_LDS);
  k1_prep<<<K1_GRID, 256, K1_LDS, stream>>>(pa);
  k2_conv<<<K2_GRID, 64, K2_LDS, stream>>>(ws, (const u16*)d_in[8], (const u16*)d_in[11]);
  k3_gemm1<<<256, 512, GEMM_LDS, stream>>>(ws);
  k4_gemm2<<<256, 512, GEMM_LDS, stream>>>(ws, (const u16*)d_in[11], (const u16*)d_in[12]);
  k5_gemm3<<<256, 512, GEMM_LDS, stream>>>(ws, (u16*)d_out);
}
```

```cpp
#include <hip/hip_runtime.h>
#include <hip/hip_bf16.h>

typedef unsigned short u16;
typedef short bf16x8 __attribute__((ext_vector_type(8)));
typedef short s16x4 __attribute__((ext_vector_type(4)));
typedef float f32x4 __attribute__((ext_vector_type(4)));
typedef float f32x2 __attribute__((ext_vector_type(2)));
typedef unsigned u32x2 __attribute__((ext_vector_type(2)));
typedef unsigned u32x4 __attribute__((ext_vector_type(4)));
typedef __bf16 bf2_t __attribute__((ext_vector_type(2)));
typedef __amdgpu_buffer_rsrc_t rsrc_t;
#define LAS __attribute__((address_space(3)))
#define GAS __attribute__((address_space(1)))
typedef GAS unsigned gu32;
typedef GAS unsigned long long gu64;
#define RLX_AGENT __ATOMIC_RELAXED, __HIP_MEMORY_SCOPE_AGENT

constexpr int L_ = 2048, D_ = 576, N_ = 128, FH = 2304, F_ = 4608;

constexpr size_t WS_EPOCH = 0;
constexpr size_t WS_ST1 = 4096;
constexpr size_t WS_FLAGA = WS_ST1 + 16384;
constexpr size_t WS_FLAGB = WS_FLAGA + 16384;
constexpr size_t WS_SLOT = 65536;
constexpr size_t WS_HT = WS_SLOT + 524288;
constexpr size_t WS_W1T = WS_HT + (size_t)D_ * L_ * 2;
constexpr size_t WS_W3T = WS_W1T + (size_t)D_ * F_ * 2;
constexpr size_t WS_CSP = WS_W3T + (size_t)FH * D_ * 2;
constexpr size_t WS_Z2T = WS_CSP + 18 * F_ * 4;
constexpr size_t WS_GZ = WS_Z2T + (size_t)L_ * FH * 2;

struct Args {
  const u16 *x, *are, *aim, *bre, *bim, *cre, *cim, *logdt, *dskip, *win, *wsp, *bsp, *wout;
  u16* out;
  char* ws;
};

#define WAITV(n) asm volatile("s_waitcnt vmcnt(%0)" ::"n"(n) : "memory")
#define WAITL(n) asm volatile("s_waitcnt lgkmcnt(%0)" ::"n"(n) : "memory")
#define BAR() __builtin_amdgcn_s_barrier()
#define LBAR() do { WAITL(0); __builtin_amdgcn_s_barrier(); asm volatile("" ::: "memory"); } while (0)
#define SCHED() __builtin_amdgcn_sched_barrier(0)
constexpr unsigned SPIN_CAP = 1u << 22;
#define DST(idx)

__device__ __forceinline__ unsigned cvtpk(float lo, float hi) {
  const bf2_t r = __builtin_convertvector((f32x2){lo, hi}, bf2_t);
  return __builtin_bit_cast(unsigned, r);
}
__device__ __forceinline__ float bflo(unsigned v) { return __uint_as_float(v << 16); }
__device__ __forceinline__ float bfhi(unsigned v) { return __uint_as_float(v & 0xffff0000u); }
__device__ __forceinline__ float bfr(float x) { return bflo(cvtpk(x, x)); }
__device__ __forceinline__ float bf2f(u16 v) { return __uint_as_float((unsigned)v << 16); }
__device__ __forceinline__ s16x4 trread(LAS const char* p) { return __builtin_amdgcn_ds_read_tr16_b64_v4i16((LAS s16x4*)p); }
__device__ __forceinline__ rsrc_t mkrsrc(const void* p, unsigned bytes) { return __builtin_amdgcn_make_buffer_rsrc((void*)p, 0, bytes, 0x00020000); }
__device__ __forceinline__ void st128(u32x4 v, rsrc_t r, unsigned voff) { __builtin_amdgcn_raw_buffer_store_b128(v, r, voff, 0, 16); }
__device__ __forceinline__ void st64(u32x2 v, rsrc_t r, unsigned voff) { __builtin_amdgcn_raw_buffer_store_b64(v, r, voff, 0, 16); }
__device__ __forceinline__ u32x4 ld128(rsrc_t r, unsigned voff, int soff) { return __builtin_amdgcn_raw_buffer_load_b128(r, voff, soff, 16); }
__device__ __forceinline__ f32x2 ln_scalars(float mean, float var) {
  const float mb = bfr(mean), vb = bfr(fmaxf(var, 0.f));
  return (f32x2){mb, bfr(__builtin_amdgcn_sqrtf(bfr(vb + 1e-5f)))};
}
template <int CTRL> __device__ __forceinline__ float dpp(float v) {
  return __builtin_bit_cast(float, __builtin_amdgcn_update_dpp(0, __builtin_bit_cast(int, v), CTRL, 0xf, 0xf, false));
}
template <int SC1> __device__ __forceinline__ void dma1k(LAS char* dst, rsrc_t r, unsigned voff, int soff) {
  const unsigned m = (unsigned)(size_t)dst;
  if (SC1) asm volatile("s_mov_b32 m0, %0\n\ts_nop 0\n\tbuffer_load_dwordx4 %1, %2, %3 offen sc1 lds" :: "s"(m), "v"(voff), "s"(r), "s"(soff) : "memory");
  else asm volatile("s_mov_b32 m0, %0\n\ts_nop 0\n\tbuffer_load_dwordx4 %1, %2, %3 offen lds" :: "s"(m), "v"(voff), "s"(r), "s"(soff) : "memory");
}
__device__ __forceinline__ void flag_a(char* ws, int idx, unsigned tag, int lane) {
  if (lane < 8) __hip_atomic_store((gu32*)(ws + WS_FLAGA) + lane * 512 + idx, tag, RLX_AGENT);
}

__device__ __forceinline__ void phase_prep(const Args& a, int p, int wid, unsigned tag, LAS char* sm0) {
  char* ws = a.ws;
  const int lane = threadIdx.x & 63, tid = threadIdx.x & 255, hb = wid >> 2, w = wid & 3;
  const int unit = hb * 112 + p, gw = p * 3 + wid;
  const bool do_ln = wid < 3 && gw < 256, is1 = unit < 144, is3 = !is1 && unit < 216;
  LAS char* sm = sm0 + hb * (32 * 1184);
  u32x4 xv[9];
  const int row = gw * 8 + (lane >> 3);
  if (do_ln) {
    const u32x4* src = (const u32x4*)(a.x + (size_t)row * D_) + (lane & 7);
#pragma unroll
    for (int i = 0; i < 9; ++i) xv[i] = src[8 * i];
  }
  const int kp = unit - 144, P = kp / 9, pi = kp - P * 9;
  const u16* src = is1 ? a.win + (unit & 7) * 576 : a.wout;
  const int ld = is1 ? F_ : D_;
  const int r0 = is1 ? (unit >> 3) * 32 : (pi < 8 ? 288 * P + 144 * (pi >> 2) + 32 * (pi & 3) : 288 * P + 128);
  const int r1 = (is1 || pi < 8) ? r0 + 16 : r0 + 144;
  u32x4 v[9];
  if (is1 || is3) {
#pragma unroll
    for (int i = 0; i < 9; ++i) {
      const int id = i * 256 + tid, rr = id / 72, c = id - rr * 72;
      v[i] = *(const u32x4*)(src + (size_t)(rr < 16 ? r0 + rr : r1 + rr - 16) * ld + c * 8);
    }
  }
  if (do_ln) {
    float s = 0.f;
#pragma unroll
    for (int i = 0; i < 9; ++i)
#pragma unroll
      for (int j = 0; j < 4; ++j) s += bflo(xv[i][j]) + bfhi(xv[i][j]);
    s += dpp<0xB1>(s); s += dpp<0x4E>(s); s += dpp<0x141>(s);
    const float mean = s * (1.0f / D_);
    float q = 0.f;
#pragma unroll
    for (int i = 0; i < 9; ++i)
#pragma unroll
      for (int j = 0; j < 4; ++j) { const float d0 = bflo(xv[i][j]) - mean, d1 = bfhi(xv[i][j]) - mean; q += d0 * d0 + d1 * d1; }
    q += dpp<0xB1>(q); q += dpp<0x4E>(q); q += dpp<0x141>(q);
    const f32x2 sc = ln_scalars(mean, q * (1.0f / D_));
    if ((lane & 7) == 0)
      __hip_atomic_store((gu64*)(ws + WS_ST1) + row, ((unsigned long long)tag << 32) | (__float_as_uint(sc.y) & 0xffff0000u) | (__float_as_uint(sc.x) >> 16), RLX_AGENT);
  }
  DST(1);
  if (is1 || is3) {
#pragma unroll
    for (int i = 0; i < 9; ++i) { const int id = i * 256 + tid, rr = id / 72, c = id - rr * 72; *(LAS u32x4*)(sm + rr * 1184 + c * 16) = v[i]; }
  }
  LBAR();
  const int g = lane >> 4, fr = lane & 15, q = fr >> 2, pp = lane & 3;
  if (is1) {
    const int ks = unit >> 3, cg = unit & 7;
    LAS const char* rp = sm + (8 * g + q) * 1184 + w * 288 + pp * 8;
    const rsrc_t rd = mkrsrc(ws + WS_W1T + ((size_t)(cg * 4 + w) * 18 + ks) * 9216, 9216);
    gu32* cs = (gu32*)(ws + WS_CSP) + ks * F_ + cg * 576 + w * 144 + fr;
#pragma unroll
    for (int nt = 0; nt < 9; ++nt) {
      const s16x4 a0 = trread(rp + nt * 32), a1 = trread(rp + nt * 32 + 4 * 1184);
      const u32x4 f = __builtin_bit_cast(u32x4, __builtin_shufflevector(a0, a1, 0, 1, 2, 3, 4, 5, 6, 7));
      st128(f, rd, lane * 16 + nt * 1024);
      float s = 0.f;
#pragma unroll
      for (int j = 0; j < 4; ++j) s += bflo(f[j]) + bfhi(f[j]);
      s += __shfl_xor(s, 16); s += __shfl_xor(s, 32);
      if (g == 0) __hip_atomic_store(cs + nt * 16, __float_as_uint(s), RLX_AGENT);
    }
  } else if (is3) {
#pragma unroll
    for (int tt = 0; tt < 2; ++tt) {
      const int tc = w * 2 + tt;
      LAS const char* rp = sm + (4 * g + q) * 1184 + tc * 144 + pp * 8;
      const rsrc_t rd = mkrsrc(ws + WS_W3T + ((size_t)tc * 72 + kp) * 4608, 4608);
#pragma unroll
      for (int nt = 0; nt < 5; ++nt) {
        const s16x4 a0 = trread(rp + nt * 32), a1 = trread(rp + nt * 32 + 16 * 1184);
        const u32x4 f = __builtin_bit_cast(u32x4, __builtin_shufflevector(a0, a1, 0, 1, 2, 3, 4, 5, 6, 7));
        st128(f, rd, nt < 4 ? nt * 1024 + lane * 16 : (fr < 8 ? 4096 + g * 128 + fr * 16 : 0x10000));
      }
    }
  }
  WAITV(0);
  LBAR();
  if (w == 0 && (is1 || is3)) flag_a(ws, 144 + unit, tag, lane);
  DST(2);
  const rsrc_t rs = mkrsrc(a.wsp, L_ * L_ * 2);
#pragma unroll
  for (int k = 0; k < 10; ++k) dma1k<0>(sm0 + wid * 1024, rs, lane * 16, (p * 8 + wid + 896 * k) * 1024);
  WAITV(0);
}

constexpr int CP = 1024, CPS = 8256;
constexpr int CU = CP + 4 * CPS, CUS = 4608;
constexpr int CX = CU + 4 * CUS;
constexpr int CPRM = CX + 16384;
constexpr int CPART = CPRM + 8192;
constexpr int CKT = CPART + 24576, CKS = 4112;
constexpr int CEX = CPRM;

__device__ __forceinline__ int kg_l0(int T, int r) {
  if (T == 0) return 128 * (r >> 3) + (r & 7);
  if (T == 1) return r < 8 ? 256 + 128 * (r >> 2) + 2 * (r & 3) : 512 + 128 * ((r - 8) >> 1) + 4 * (r & 1);
  return r <= 8 ? 1024 + 128 * r : 0;
}
__device__ __forceinline__ void cexp_rev(float zr2, float zir, float l, float& re, float& im) {
  const float m = __builtin_amdgcn_exp2f(zr2 * l);
  const float ph = __builtin_amdgcn_fractf(zir * l);
  re = m * __builtin_amdgcn_cosf(ph); im = m * __builtin_amdgcn_sinf(ph);
}
__device__ __forceinline__ void split2(float a, float b, unsigned& hi, unsigned& lo) {
  hi = cvtpk(a, b); lo = cvtpk(a - bflo(hi), b - bfhi(hi));
}

__device__ __forceinline__ void phase_conv(const Args& a, int cgp, int wid, unsigned tag, LAS char* sm) {
  char* ws = a.ws;
  const int tid = threadIdx.x, lane = tid & 63, d0 = cgp * 4;
  {
    const int i = d0 * N_ + tid;
    const u16 vare = a.are[i], vaim = a.aim[i], vbre = a.bre[i], vbim = a.bim[i], vcre = a.cre[i], vcim = a.cim[i], vld = a.logdt[d0 + (tid >> 7)];
    const float dt = bfr(__expf(bf2f(vld)));
    const float zr = dt * bf2f(vare), zi = dt * bf2f(vaim), br = dt * bf2f(vbre), bi = dt * bf2f(vbim), cr = bf2f(vcre), ci = bf2f(vcim);
    *(LAS f32x4*)(sm + CPRM + tid * 16) = (f32x4){zr * 1.44269504f, zi * 0.15915494f, cr * br - ci * bi, cr * bi + ci * br};
  }
  u32x2 xg[4];
#pragma unroll
  for (int i = 0; i < 4; ++i) xg[i] = *(const u32x2*)(a.x + (size_t)(tid * 4 + i) * D_ + d0);
  const float dsk = bf2f(a.dskip[d0 + (wid & 3)]);
  if (tid < 64) *(LAS unsigned*)(sm + CP + (tid >> 4) * CPS + 8192 + (tid & 15) * 4) = 0u;
  *(LAS unsigned*)(sm + CU + (tid >> 7) * CUS + ((tid >> 6) & 1) * 2304 + (tid & 63) * 4) = 0u;
  LBAR();
  DST(1);
  const int r = lane & 15, kg = lane >> 4;
  {
    const float l0f[3] = {(float)kg_l0(0, r), (float)kg_l0(1, r), (float)kg_l0(2, r)};
    const float lb = (float)(8 * r);
    f32x4 acc[3] = {};
#pragma unroll 1
    for (int kk = 0; kk < 4; ++kk) {
      u32x4 ah[3], al[3], bh, bl;
#pragma unroll
      for (int i = 0; i < 4; ++i) {
        const f32x4 pp = *(LAS const f32x4*)(sm + CPRM + (wid * 64 + kk * 16 + kg * 4 + i) * 16);
        float re, im;
#pragma unroll
        for (int T = 0; T < 3; ++T) { cexp_rev(pp[0], pp[1], l0f[T], re, im); unsigned h, l; split2(re, im, h, l); ah[T][i] = h; al[T][i] = l; }
        cexp_rev(pp[0], pp[1], lb, re, im);
        const float fr = re * pp[2] - im * pp[3], fi = re * pp[3] + im * pp[2];
        unsigned h, l; split2(fr, -fi, h, l); bh[i] = h; bl[i] = l;
      }
#pragma unroll
      for (int T = 0; T < 3; ++T) {
        acc[T] = __builtin_amdgcn_mfma_f32_16x16x32_bf16(__builtin_bit_cast(bf16x8, al[T]), __builtin_bit_cast(bf16x8, bh), acc[T], 0, 0, 0);
        acc[T] = __builtin_amdgcn_mfma_f32_16x16x32_bf16(__builtin_bit_cast(bf16x8, ah[T]), __builtin_bit_cast(bf16x8, bl), acc[T], 0, 0, 0);
        acc[T] = __builtin_amdgcn_mfma_f32_16x16x32_bf16(__builtin_bit_cast(bf16x8, ah[T]), __builtin_bit_cast(bf16x8, bh), acc[T], 0, 0, 0);
      }
    }
#pragma unroll
    for (int T = 0; T < 3; ++T)
#pragma unroll
      for (int j = 0; j < 4; ++j) *(LAS float*)(sm + CPART + (wid * 768 + T * 256 + (kg * 4 + j) * 16 + r) * 4) = acc[T][j];
  }
  const rsrc_t rst = mkrsrc(ws + WS_ST1, 16384);
  u32x4 g0 = ld128(rst, tid * 32, 0), g1 = ld128(rst, tid * 32 + 16, 0);
  LBAR();
  DST(2);
  {
    const int ch = tid >> 7, t7 = tid & 127;
#pragma unroll
    for (int i = 0; i < 6; ++i) {
      const int e = i * 128 + t7, T = i >> 1, row = (e >> 4) & 15, b = e & 15;
      const float k = *(LAS const float*)(sm + CPART + (ch * 1536 + e) * 4) + *(LAS const float*)(sm + CPART + (ch * 1536 + 768 + e) * 4);
      const int l = kg_l0(T, row) + 8 * b;
      if (T < 2 || row < 8 || (row == 8 && b == 0)) *(LAS u16*)(sm + CKT + ch * CKS + l * 2) = (u16)(cvtpk(k, k) & 0xffffu);
    }
    LBAR();
    unsigned kv[17];
#pragma unroll
    for (int i = 0; i < 17; ++i) {
      const int T = t7 * 16 - 1 + i;
      const int rho = (int)bfr((float)(T < 0 ? 0 : T));
      kv[i] = T < 0 ? 0u : (unsigned)*(LAS const u16*)(sm + CKT + ch * CKS + rho * 2);
    }
#pragma unroll
    for (int c = 0; c < 4; ++c) {
      u32x4 o;
#pragma unroll
      for (int i = 0; i < 4; ++i) { const int j = 15 - 4 * c - i; o[i] = kv[j + 1] | (kv[j] << 16); }
      *(LAS u32x4*)(sm + CP + ch * CPS + (2032 - t7 * 16 + 4 * c) * 4) = o;
    }
  }
  DST(3);
  for (unsigned spins = 0; spins < SPIN_CAP; ++spins) {
    if (__all(g0[1] == tag && g0[3] == tag && g1[1] == tag && g1[3] == tag)) break;
    __builtin_amdgcn_s_sleep(1);
    g0 = ld128(rst, tid * 32, 0); g1 = ld128(rst, tid * 32 + 16, 0);
  }
  {
    const unsigned sv[4] = {g0[0], g0[2], g1[0], g1[2]};
    unsigned uu[4][2];
#pragma unroll
    for (int i = 0; i < 4; ++i) {
      const float m = bflo(sv[i]), rs = __builtin_amdgcn_rcpf(bfhi(sv[i]));
#pragma unroll
      for (int j = 0; j < 2; ++j) {
        const unsigned dd = cvtpk(bflo(xg[i][j]) - m, bfhi(xg[i][j]) - m);
        uu[i][j] = cvtpk(bflo(dd) * rs, bfhi(dd) * rs);
      }
    }
#pragma unroll
    for (int c = 0; c < 4; ++c) {
      const unsigned sel = (c & 1) ? 0x07060302u : 0x05040100u;
      const u32x2 pu = {__builtin_amdgcn_perm(uu[1][c >> 1], uu[0][c >> 1], sel), __builtin_amdgcn_perm(uu[3][c >> 1], uu[2][c >> 1], sel)};
      const u32x2 px = {__builtin_amdgcn_perm(xg[1][c >> 1], xg[0][c >> 1], sel), __builtin_amdgcn_perm(xg[3][c >> 1], xg[2][c >> 1], sel)};
      *(LAS u32x2*)(sm + CU + c * CUS + 256 + ((tid >> 1) & 1) * 2304 + (tid >> 2) * 16 + (tid & 1) * 8) = pu;
      *(LAS u32x2*)(sm + CX + c * 4096 + tid * 8) = px;
    }
  }
  LBAR();
  DST(4);
  const int ch = wid & 3, hf = wid >> 2, n = r;
  f32x4 acc[8] = {};
  {
    LAS const char* pa = sm + CP + ch * CPS + 4 * (2047 - n - 16 * (kg & 1) + 8 * (kg >> 1) - 32 * 63) - hf * 512;
    LAS const char* pb = sm + CU + ch * CUS + 256 + (kg >> 1) * 2304 + 16 * (n - (kg & 1)) - hf * 128;
#pragma unroll
    for (int rr = 0; rr < 4; ++rr) {
      bf16x8 A[8], B[8];
#pragma unroll
      for (int c = 0; c < 8; ++c) {
        LAS const unsigned* q = (LAS const unsigned*)(pa + (63 - 8 * c - rr) * 128);
        A[c] = __builtin_bit_cast(bf16x8, (u32x4){q[0], q[2], q[4], q[6]});
        B[c] = *(LAS const bf16x8*)(pb + (8 * c - rr) * 32);
      }
#pragma unroll
      for (int c = 0; c < 8; ++c)
#pragma unroll
        for (int e = 0; e + c < 8; ++e) acc[c + e] = __builtin_amdgcn_mfma_f32_16x16x32_bf16(A[c], B[e], acc[c + e], 0, 0, 0);
    }
  }
#pragma unroll
  for (int t = 0; t < 4; ++t) *(LAS f32x4*)(sm + CEX + ((wid * 4 + t) * 64 + lane) * 16) = hf ? acc[t] : acc[4 + t];
  LBAR();
  DST(5);
  {
    const rsrc_t rh = mkrsrc(ws + WS_HT + (size_t)(d0 + ch) * 4096 + hf * 2048, 2048);
    LAS const char* pu = sm + CU + ch * CUS + 256 + (kg >> 1) * 2304 + hf * 1024 + 16 * n + 8 * (kg & 1);
    LAS const char* px = sm + CX + ch * 4096 + hf * 2048 + n * 32 + kg * 8;
    u32x2 o[4];
#pragma unroll
    for (int t = 0; t < 4; ++t) {
      const f32x4 y4 = (hf ? acc[4 + t] : acc[t]) + *(LAS const f32x4*)(sm + CEX + (((wid ^ 4) * 4 + t) * 64 + lane) * 16);
      const u32x2 uv = *(LAS const u32x2*)(pu + t * 256), xv = *(LAS const u32x2*)(px + t * 512);
#pragma unroll
      for (int j = 0; j < 2; ++j) {
        const unsigned y = cvtpk(y4[2 * j], y4[2 * j + 1]);
        const unsigned sk = cvtpk(bflo(uv[j]) * dsk, bfhi(uv[j]) * dsk);
        const unsigned s = cvtpk(bflo(y) + bflo(sk), bfhi(y) + bfhi(sk));
        o[t][j] = cvtpk(bflo(xv[j]) + bflo(s), bfhi(xv[j]) + bfhi(s));
      }
    }
#pragma unroll
    for (int t = 0; t < 4; t += 2) {
      const auto s0 = __builtin_amdgcn_permlane16_swap(o[t][0], o[t + 1][0], false, false);
      const auto s1 = __builtin_amdgcn_permlane16_swap(o[t][1], o[t + 1][1], false, false);
      st128((u32x4){s0[0], s1[0], s0[1], s1[1]}, rh, (t + (kg & 1)) * 512 + n * 32 + (kg >> 1) * 16);
    }
  }
  WAITV(0);
  LBAR();
  if (wid == 0) flag_a(ws, cgp, tag, lane);
}

constexpr int STG = 272 * 128;
constexpr int RING = 4 * STG;
constexpr int T_CS = RING, T_LN2 = RING + 1152, T_LN3 = RING + 2176, T_H = RING + 4096;
constexpr int LDS_BYTES = RING + 4096 + 80 * 128;
constexpr int NS3 = 7;

template <int SC1> __device__ __forceinline__ void issue_a(LAS char* dst, int w, rsrc_t ra, unsigned va, int sa, int pa) {
#pragma unroll
  for (int i = 0; i < 4; ++i) dma1k<SC1>(dst + (4 * i + w) * 1024, ra, va, sa + (4 * i + w) * pa);
}
__device__ __forceinline__ void issue_b(LAS char* dst, int w, rsrc_t rb, unsigned vb, int sb) {
#pragma unroll
  for (int i = 0; i < 4; ++i) dma1k<1>(dst + (4 * i + w) * 1024, rb, vb, sb + (4 * i + w) * 1024);
  if (w < 2) dma1k<1>(dst + (16 + w) * 1024, rb, vb, sb + (16 + w) * 1024);
}
template <int NA, int NB, int NX = 0> __device__ __forceinline__ void wait_parts(int w) {
  if (w < 2) WAITV(NA * 4 + NB * 5 + NX); else WAITV(NA * 4 + NB * 4 + NX);
}
__device__ __forceinline__ f32x2 gelu2(f32x2 x) {
  const f32x2 a = x * ((x * x) * (-2.3022082f * 0.044715f) + (-2.3022082f));
  f32x2 e; e.x = __builtin_amdgcn_exp2f(a.x); e.y = __builtin_amdgcn_exp2f(a.y);
  e = e + 1.0f;
  f32x2 r; r.x = __builtin_amdgcn_rcpf(e.x); r.y = __builtin_amdgcn_rcpf(e.y);
  return x * r;
}

#define MMX(F) do { _Pragma("unroll") for (int nt = 0; nt < 9; ++nt) _Pragma("unroll") for (int mt = 0; mt < 2; ++mt) \
    acc[mt][nt] = __builtin_amdgcn_mfma_f32_16x16x32_bf16(F[mt], F[2 + nt], acc[mt][nt], 0, 0, 0); } while (0)
#define MMY(F) do { _Pragma("unroll") for (int nt = 0; nt < 9; ++nt) _Pragma("unroll") for (int mt = 0; mt < 2; ++mt) \
    acc[mt][nt] = __builtin_amdgcn_mfma_f32_16x16x32_bf16(F[2 + nt], F[mt], acc[mt][nt], 0, 0, 0); } while (0)
#define RDB(F, so, ks) do { _Pragma("unroll") for (int nt = 0; nt < 9; ++nt) \
    F[2 + nt] = *(const LAS bf16x8*)(sm + (so) + boff + ((ks) * 9 + nt) * 1024); } while (0)
#define RDA(F, so, ks) do { _Pragma("unroll") for (int mt = 0; mt < 2; ++mt) \
    F[mt] = *(const LAS bf16x8*)(sm + (so) + (aoff2 ^ ((ks) * 64)) + mt * 2048); } while (0)
#define RDAT(F, so, ks, MTX) do { _Pragma("unroll") for (int mt = 0; mt < 2; ++mt) { \
    const s16x4 t0_ = trread(sm + (so) + (aoff ^ (mt * (MTX))) + (ks) * 8192), t1_ = trread(sm + (so) + (aoff ^ (mt * (MTX))) + (ks) * 8192 + 1024); \
    F[mt] = __builtin_shufflevector(t0_, t1_, 0, 1, 2, 3, 4, 5, 6, 7); } } while (0)
#define ILV(NR) do { _Pragma("unroll") for (int q_ = 0; q_ < (NR); ++q_) { __builtin_amdgcn_sched_group_barrier(0x100, 1, 0); __builtin_amdgcn_sched_group_barrier(0x8, 1, 0); } \
    __builtin_amdgcn_sched_group_barrier(0x8, 18 - (NR), 0); } while (0)
#define ZERO_ACC() do { _Pragma("unroll") for (int mt = 0; mt < 2; ++mt) _Pragma("unroll") for (int nt = 0; nt < 9; ++nt) acc[mt][nt] = (f32x4){0.f, 0.f, 0.f, 0.f}; } while (0)

__global__ __launch_bounds__(512, 2) void fused_block(Args a) {
  extern __shared__ __attribute__((aligned(16))) char smem[];
  LAS char* sm = (LAS char*)smem;
  char* ws = a.ws;
  const int wid = __builtin_amdgcn_readfirstlane(threadIdx.x >> 6), w = wid & 3;
  const int xcd = blockIdx.x & 7, idx = blockIdx.x >> 3;
  DST(0);
  const unsigned tag = __builtin_amdgcn_readfirstlane(__hip_atomic_load((gu32*)(ws + WS_EPOCH), RLX_AGENT)) + 1u;
  if (idx < 18) phase_conv(a, xcd * 18 + idx, wid, tag, sm);
  else phase_prep(a, xcd * 14 + idx - 18, wid, tag, sm);
  DST(6);
  int lane = threadIdx.x & 63;
  asm volatile("" : "+v"(lane));
  if (wid == 4) {
    const rsrc_t rf = mkrsrc(ws + WS_FLAGA + xcd * 2048, 360 * 4);
    for (unsigned spins = 0; spins < SPIN_CAP; ++spins) {
      const u32x4 f0 = ld128(rf, lane * 16, 0), f1 = ld128(rf, 1024 + lane * 16, 0);
      const bool ok = f0[0] == tag && f0[1] == tag && f0[2] == tag && f0[3] == tag &&
                      (lane >= 26 || (f1[0] == tag && f1[1] == tag && f1[2] == tag && f1[3] == tag));
      if (__all(ok)) break;
      __builtin_amdgcn_s_sleep(2);
    }
  }
  LBAR();
  DST(7);
  int bb = blockIdx.x;
  asm volatile("" : "+s"(bb));
  const int tm = (bb & 1) * 8 + ((bb >> 3) & 7), tn = ((bb >> 1) & 3) * 4 + (bb >> 6);
  const int fr = lane & 15, kg = lane >> 4;
  const int boff = 16384 + lane * 16;
  const rsrc_t rh = mkrsrc(ws + WS_HT, D_ * L_ * 2), rw1 = mkrsrc(ws + WS_W1T, D_ * F_ * 2);
  const unsigned vh = kg * 4096 + (((fr >> 1) ^ (kg | ((w >> 1) << 2))) << 5) + (lane & 1) * 16, vl = lane * 16;
#define ISS1(s) do { issue_a<1>(sm + ((s) & 3) * STG, w, rh, vh, ((s) < 9 ? (s) : (s) - 9) * 64 * 4096 + tm * 256, 4 * 4096); \
    issue_b(sm + ((s) & 3) * STG + 16384, w, rw1, vl, ((s) < 9 ? (16 + tn) * 9 + (s) : tn * 9 + (s) - 9) * 18432); } while (0)
  gu32* flagz = (gu32*)(ws + WS_FLAGB) + tn * 64;
  gu32* flagg = (gu32*)(ws + WS_FLAGB) + 1024 + tm * 64;
  f32x4 acc[2][9];
  bf16x8 F0[11], F1[11];

  if (wid < 4) {
    const int q = fr >> 2, p = lane & 3;
    const int aoff = (8 * kg + q) * 256 + (((2 * w + (p >> 1)) ^ (q | ((kg & 1) << 2))) << 5) + (p & 1) * 16;
#define ITER(s) do { \
    RDAT(F1, ((s) & 3) * STG, 1, 8); RDB(F1, ((s) & 3) * STG, 1); MMX(F0); ILV(13); SCHED(); \
    WAITL(0); BAR(); SCHED(); \
    if ((s) < 8) { RDAT(F0, (((s) + 1) & 3) * STG, 0, 8); RDB(F0, (((s) + 1) & 3) * STG, 0); } else ISS1(12); \
    MMX(F1); if ((s) < 8) ILV(13); SCHED(); } while (0)
    {
      gu32* csp = (gu32*)(ws + WS_CSP);
      const int t = w * 64 + lane;
      float c0 = 0.f, c1 = 0.f;
      const int f0 = t < 144 ? FH + tn * 144 + t : tn * 144 + t - 144, f1 = tn * 144 + 112 + (t & 31);
#pragma unroll
      for (int k = 0; k < 18; ++k) {
        c0 += __uint_as_float(__hip_atomic_load(csp + k * F_ + f0, RLX_AGENT));
        c1 += __uint_as_float(__hip_atomic_load(csp + k * F_ + f1, RLX_AGENT));
      }
      *(LAS float*)(sm + T_CS + t * 4) = c0;
      if (t < 32) *(LAS float*)(sm + T_CS + (256 + t) * 4) = c1;
    }
    ZERO_ACC();
    BAR();
    DST(8);
    RDAT(F0, 0, 0, 8); RDB(F0, 0, 0);
    ITER(0); ITER(1); ITER(2); ITER(3); ITER(4); ITER(5); ITER(6); ITER(7); ITER(8);
#undef ITER
    DST(9);
    __builtin_amdgcn_s_setprio(2);
    const rsrc_t rs = mkrsrc(a.wsp + (size_t)tm * 128 * L_, 128 * L_ * 2), rz = mkrsrc(ws + WS_Z2T + (size_t)tn * 32 * 18432, 32 * 18432);
    const unsigned vs = (lane >> 3) * 4096 + (((lane & 7) ^ (lane >> 3)) << 4);
#define ISS2A(g) issue_a<0>(sm + (((g) + 2) & 3) * STG, w, rs, vs, (g) * 128, 8 * 4096)
#define ISS2B(g) issue_b(sm + (((g) + 2) & 3) * STG + 16384, w, rz, vl, (g) * 18432)
    f32x4 rs4[2], nm4[2], s1[2] = {}, s2[2] = {};
#pragma unroll
    for (int mt = 0; mt < 2; ++mt) {
      const f32x4 t0 = *(const LAS f32x4*)(sm + T_LN2 + (w * 32 + 8 * kg + 4 * mt) * 8), t1 = *(const LAS f32x4*)(sm + T_LN2 + (w * 32 + 8 * kg + 4 * mt) * 8 + 16);
      rs4[mt] = (f32x4){t0[0], t0[2], t1[0], t1[2]}; nm4[mt] = (f32x4){t0[1], t0[3], t1[1], t1[3]};
    }
#define EPI1(nt) do { const float c_ = *(const LAS float*)(sm + T_CS + ((nt) * 16 + fr) * 4); \
    _Pragma("unroll") for (int mt = 0; mt < 2; ++mt) { const f32x4 v_ = acc[mt][nt] * rs4[mt] + nm4[mt] * c_; \
      const f32x2 a_ = gelu2((f32x2){v_[0], v_[1]}), b_ = gelu2((f32x2){v_[2], v_[3]}); const f32x4 z_ = {a_.x, a_.y, b_.x, b_.y}; \
      acc[mt][nt] = z_; s1[mt] += z_; s2[mt] += z_ * z_; } } while (0)
#define PIN3(n0) asm volatile("" : "+v"(acc[0][n0]), "+v"(acc[1][n0]), "+v"(acc[0][n0 + 1]), "+v"(acc[1][n0 + 1]), "+v"(acc[0][n0 + 2]), "+v"(acc[1][n0 + 2]), \
                               "+v"(s1[0]), "+v"(s1[1]), "+v"(s2[0]), "+v"(s2[1]))
    EPI1(0); EPI1(1); EPI1(2); PIN3(0); SCHED(); BAR(); ISS1(13); SCHED();
    EPI1(3); EPI1(4); EPI1(5); PIN3(3); SCHED(); BAR(); ISS1(14); SCHED();
    EPI1(6); EPI1(7); EPI1(8); PIN3(6);
    const rsrc_t rsl = mkrsrc(ws + WS_SLOT + (size_t)(tm * 4 + w) * 8192, 8192);
    {
      float v8[2][8], v4[2][4], v2[2][2], v1[2];
#pragma unroll
      for (int i = 0; i < 8; ++i) { v8[0][i] = s1[i >> 2][i & 3]; v8[1][i] = s2[i >> 2][i & 3]; }
#pragma unroll
      for (int u = 0; u < 2; ++u) {
        const bool b2 = fr & 4, b1 = fr & 2, b0 = fr & 1;
#pragma unroll
        for (int i = 0; i < 4; ++i) { const float k = b2 ? v8[u][4 + i] : v8[u][i], sn = b2 ? v8[u][i] : v8[u][4 + i]; v4[u][i] = k + dpp<0x141>(dpp<0x1B>(sn)); }
#pragma unroll
        for (int i = 0; i < 2; ++i) { const float k = b1 ? v4[u][2 + i] : v4[u][i], sn = b1 ? v4[u][i] : v4[u][2 + i]; v2[u][i] = k + dpp<0x4E>(sn); }
        { const float k = b0 ? v2[u][1] : v2[u][0], sn = b0 ? v2[u][0] : v2[u][1]; v1[u] = k + dpp<0xB1>(sn); }
        v1[u] += dpp<0x128>(v1[u]);
      }
      st128((u32x4){__float_as_uint(v1[0]), tag, __float_as_uint(v1[1]), tag}, rsl, fr < 8 ? tn * 512 + (8 * kg + fr) * 16 : 0x10000);
    }
    DST(10);
    wait_parts<2, 2, 1>(w); BAR(); ISS1(15); SCHED();
    {
      u32x4 g[8];
      for (unsigned spins = 0; spins < SPIN_CAP; ++spins) {
        bool ok = true;
#pragma unroll
        for (int i = 0; i < 8; ++i) { g[i] = ld128(rsl, vl + i * 1024, 0); ok &= g[i][1] == tag && g[i][3] == tag; }
        if (__all(ok)) break;
        __builtin_amdgcn_s_sleep(1);
      }
      float s = 0.f, qq = 0.f;
#pragma unroll
      for (int i = 0; i < 8; ++i) { s += __uint_as_float(g[i][0]); qq += __uint_as_float(g[i][2]); }
      s += __shfl_xor(s, 32); qq += __shfl_xor(qq, 32);
      const float mean = s * (1.0f / FH);
      const f32x2 sc = ln_scalars(mean, qq * (1.0f / FH) - mean * mean);
      const float ri = __builtin_amdgcn_rcpf(sc.y);
      if (lane < 32) *(LAS f32x2*)(sm + T_LN3 + (w * 32 + lane) * 8) = (f32x2){ri, -sc.x * ri};
      WAITL(0);
#pragma unroll
      for (int mt = 0; mt < 2; ++mt) {
        const f32x4 t0 = *(const LAS f32x4*)(sm + T_LN3 + (w * 32 + 8 * kg + 4 * mt) * 8), t1 = *(const LAS f32x4*)(sm + T_LN3 + (w * 32 + 8 * kg + 4 * mt) * 8 + 16);
        rs4[mt] = (f32x4){t0[0], t0[2], t1[0], t1[2]}; nm4[mt] = (f32x4){t0[1], t0[3], t1[1], t1[3]};
      }
    }
    DST(11);
    BAR(); SCHED();
#pragma unroll
    for (int nt = 0; nt < 9; ++nt) {
      const f32x4 c = acc[0][nt] * rs4[0] + nm4[0], d = acc[1][nt] * rs4[1] + nm4[1];
      st128((u32x4){cvtpk(c[0], c[1]), cvtpk(c[2], c[3]), cvtpk(d[0], d[1]), cvtpk(d[2], d[3])}, rz, vl + (4 * tm + w) * 9216 + nt * 1024);
    }
    SCHED();
    ISS1(16);
    BAR(); ISS1(17);
    BAR(); ISS2A(0);
    wait_parts<3, 2>(w);
    if (lane == 0) __hip_atomic_store(flagz + 4 * tm + w, tag, RLX_AGENT);
    DST(12);
    wait_parts<2, 1, 1>(w); BAR(); ISS2A(1);
    wait_parts<2, 0, 1>(w); BAR(); ISS2A(2);
    BAR(); ISS2A(3);
    __builtin_amdgcn_s_setprio(0);
    DST(13);
    for (unsigned spins = 0; spins < SPIN_CAP; ++spins) {
      if (__all(__hip_atomic_load(flagz + lane, RLX_AGENT) == tag)) break;
      __builtin_amdgcn_s_sleep(1);
    }
    __builtin_amdgcn_fence(__ATOMIC_ACQUIRE, "wavefront");
    DST(14);
    ISS2B(0); ISS2B(1); ISS2B(2); ISS2B(3);
    wait_parts<0, 3>(w); BAR();
    wait_parts<0, 2>(w); BAR(); ISS2A(4); ISS2B(4);
    wait_parts<1, 2>(w); BAR(); ISS2A(5); ISS2B(5);
    for (int g = 2; g < 28; ++g) { wait_parts<2, 2>(w); BAR(); ISS2A(g + 4); ISS2B(g + 4); }
    const rsrc_t rw3 = mkrsrc(ws + WS_W3T + (size_t)(tn & 7) * 72 * 4608, 72 * 4608);
#define SL3(j) (((j) + 4) % NS3)
#define ISS3(s, j) issue_b(sm + (j) * 18432, w, rw3, vl, (s) * 18432)
    {
      const unsigned vt = (lane >> 3) * 4096 + (lane & 7) * 16;
      const int st = (tn & 7) * 72 * 4096 + tm * 256 + (tn >> 3) * 128;
      dma1k<1>(sm + T_H + w * 1024, rh, vt, st + w * 32768);
      dma1k<1>(sm + T_H + (w + 4) * 1024, rh, vt, st + (w + 4) * 32768);
      dma1k<1>(sm + T_H + 8 * 1024, rh, vt, st + 8 * 32768);
    }
    wait_parts<2, 2, 3>(w); BAR(); ISS3(0, 4);
    wait_parts<1, 2, 3>(w); BAR(); ISS3(1, 5); ISS3(2, 6);
    wait_parts<0, 3>(w); BAR(); ISS3(3, 0);
    BAR();
    BAR();
    ISS3(4, 1); ISS3(5, 2); ISS3(6, 3);
    wait_parts<0, NS3 - 1>(w); BAR();
    int slot = 4;
    for (int s = 0; s < 18 - NS3; ++s) {
      wait_parts<0, NS3 - 2>(w); BAR();
      ISS3(s + NS3, slot);
      slot = slot + 1 == NS3 ? 0 : slot + 1;
    }
    wait_parts<0, 5>(w); BAR(); wait_parts<0, 4>(w); BAR(); wait_parts<0, 3>(w); BAR();
    wait_parts<0, 2>(w); BAR(); wait_parts<0, 1>(w); BAR(); wait_parts<0, 0>(w); BAR();
    if (blockIdx.x == 0 && w == 0 && lane == 0) __hip_atomic_store((gu32*)(ws + WS_EPOCH), tag, RLX_AGENT);
    return;
  }

  const int aoff = (8 * kg + (fr >> 2)) * 256 + (((2 * w) ^ ((fr >> 2) | ((kg & 1) << 2))) << 5) + (lane & 3) * 8;
  float rs2[2], nm2[2];
  {
    ISS1(0); ISS1(1); ISS1(2); ISS1(3);
    f32x4 sum[2] = {}, sq[2] = {};
    const bf16x8 ones = {0x3f80, 0x3f80, 0x3f80, 0x3f80, 0x3f80, 0x3f80, 0x3f80, 0x3f80};
#define STATS(s) do { RDAT(F0, ((s) & 3) * STG, 0, 32); RDAT(F1, ((s) & 3) * STG, 1, 32); \
      _Pragma("unroll") for (int mt = 0; mt < 2; ++mt) { \
        sum[mt] = __builtin_amdgcn_mfma_f32_16x16x32_bf16(ones, F0[mt], sum[mt], 0, 0, 0); sq[mt] = __builtin_amdgcn_mfma_f32_16x16x32_bf16(F0[mt], F0[mt], sq[mt], 0, 0, 0); \
        sum[mt] = __builtin_amdgcn_mfma_f32_16x16x32_bf16(ones, F1[mt], sum[mt], 0, 0, 0); sq[mt] = __builtin_amdgcn_mfma_f32_16x16x32_bf16(F1[mt], F1[mt], sq[mt], 0, 0, 0); } \
      SCHED(); } while (0)
    wait_parts<3, 3>(w); BAR();
#pragma unroll
    for (int s = 0; s < 8; ++s) { STATS(s); wait_parts<2, 2>(w); WAITL(0); BAR(); ISS1(s + 4); }
    STATS(8);
#undef STATS
#pragma unroll
    for (int mt = 0; mt < 2; ++mt) {
      const float q = (fr & 2) ? ((fr & 1) ? sq[mt][3] : sq[mt][2]) : ((fr & 1) ? sq[mt][1] : sq[mt][0]);
      const float mean = sum[mt][0] * (1.0f / D_);
      const f32x2 sc = ln_scalars(mean, q * (1.0f / D_) - mean * mean);
      const float ri = __builtin_amdgcn_rcpf(sc.y);
      if (kg == (fr >> 2)) *(LAS f32x2*)(sm + T_LN2 + (w * 32 + mt * 16 + fr) * 8) = (f32x2){ri, -sc.x * ri};
    }
    wait_parts<2, 2>(w); WAITL(0); BAR();
  }
  DST(16);
#define ITER(s) do { \
    RDAT(F1, ((s) & 3) * STG, 1, 32); RDB(F1, ((s) & 3) * STG, 1); MMY(F0); ILV(13); SCHED(); \
    if ((s) == 9) wait_parts<1, 1>(w); if ((s) == 10) WAITV(0); \
    WAITL(0); BAR(); SCHED(); \
    if ((s) < 17) { RDAT(F0, (((s) + 1) & 3) * STG, 0, 32); RDB(F0, (((s) + 1) & 3) * STG, 0); } \
    MMY(F1); if ((s) < 17) ILV(13); SCHED(); } while (0)
  ZERO_ACC();
  RDAT(F0, (9 & 3) * STG, 0, 32); RDB(F0, (9 & 3) * STG, 0);
  ITER(9); ITER(10); ITER(11); ITER(12); ITER(13); ITER(14); ITER(15); ITER(16); ITER(17);
#undef ITER
  DST(17);
  const float b0 = bf2f(a.bsp[tm * 128 + w * 32 + fr]), b1 = bf2f(a.bsp[tm * 128 + w * 32 + 16 + fr]);
#pragma unroll
  for (int mt = 0; mt < 2; ++mt) { const f32x2 t = *(const LAS f32x2*)(sm + T_LN2 + (w * 32 + mt * 16 + fr) * 8); rs2[mt] = t.x; nm2[mt] = t.y; }
  u32x4 z1[9];
#pragma unroll
  for (int nt = 0; nt < 9; ++nt) {
    const f32x4 c4 = *(const LAS f32x4*)(sm + T_CS + (144 + nt * 16 + kg * 4) * 4);
#pragma unroll
    for (int mt = 0; mt < 2; ++mt) {
      const f32x4 v = acc[mt][nt] * rs2[mt] + c4 * nm2[mt];
      const f32x2 c = gelu2((f32x2){v[0], v[1]}), d = gelu2((f32x2){v[2], v[3]});
      z1[nt][2 * mt] = cvtpk(c.x, c.y); z1[nt][2 * mt + 1] = cvtpk(d.x, d.y);
    }
    asm volatile("" : "+v"(z1[nt]));
  }
  DST(18);
  {
    const int aoff2 = (w * 32 + fr) * 128 + ((kg ^ (lane & 7)) << 4);
#define ITER(slot, LAST) do { \
    RDA(F1, (slot) * STG, 1); RDB(F1, (slot) * STG, 1); MMY(F0); ILV(11); SCHED(); \
    WAITL(0); BAR(); SCHED(); \
    if (!(LAST)) { RDA(F0, (((slot) + 1) & 3) * STG, 0); RDB(F0, (((slot) + 1) & 3) * STG, 0); } \
    MMY(F1); if (!(LAST)) ILV(11); SCHED(); } while (0)
    ZERO_ACC();
    BAR();
    DST(19);
    RDA(F0, 2 * STG, 0); RDB(F0, 2 * STG, 0);
    for (int g = 0; g < 28; g += 4) { ITER(2, false); ITER(3, false); ITER(0, false); ITER(1, false); }
    ITER(2, false); ITER(3, false); ITER(0, false); ITER(1, true);
#undef ITER
  }
  DST(20);
  const rsrc_t rg = mkrsrc(ws + WS_GZ + (size_t)tm * 8 * 73728, 8 * 73728);
  {
    const int k0 = 9 * (tn >> 1);
#pragma unroll
    for (int mt = 0; mt < 2; ++mt) {
      const float b = mt ? b1 : b0;
      unsigned o[18];
#pragma unroll
      for (int nt = 0; nt < 9; ++nt)
#pragma unroll
        for (int j = 0; j < 2; ++j) {
          const unsigned z = z1[nt][2 * mt + j];
          o[2 * nt + j] = cvtpk(bflo(z) * (acc[mt][nt][2 * j] + b), bfhi(z) * (acc[mt][nt][2 * j + 1] + b));
        }
      const int sb = (2 * w + mt) * 73728 + k0 * 1024;
#pragma unroll
      for (int i = 0; i < 4; ++i) st128((u32x4){o[4 * i], o[4 * i + 1], o[4 * i + 2], o[4 * i + 3]}, rg, vl + sb + (tn & 1) * 4096 + i * 1024);
      st64((u32x2){o[16], o[17]}, rg, vl + sb + 8192 + (tn & 1) * 8);
    }
  }
  SCHED();
  BAR();
  WAITV(0);
  if (lane == 0) __hip_atomic_store(flagg + 4 * tn + w, tag, RLX_AGENT);
  DST(21);
  const int rt = 4 * (tn >> 3) + w;
  for (unsigned spins = 0; spins < SPIN_CAP; ++spins) {
    if (__all(__hip_atomic_load(flagg + 4 * fr + (rt >> 1), RLX_AGENT) == tag)) break;
    __builtin_amdgcn_s_sleep(1);
  }
  __builtin_amdgcn_fence(__ATOMIC_ACQUIRE, "wavefront");
  DST(22);
  {
    f32x4 c3[5] = {};
    bf16x8 G[12];
#define LDG(i, k) G[i] = __builtin_bit_cast(bf16x8, ld128(rg, vl, rt * 73728 + (k) * 1024))
#pragma unroll
    for (int i = 0; i < 12; ++i) LDG(i, i);
    const int b4 = 4096 + kg * 128 + (fr & 7) * 16;
    int so = 4 * 18432;
    BAR();
    bf16x8 B0[10], B1[10];
#define RD3(B, hf) do { _Pragma("unroll") for (int kk = 0; kk < 2; ++kk) { \
      _Pragma("unroll") for (int nt = 0; nt < 4; ++nt) B[kk * 5 + nt] = *(const LAS bf16x8*)(sm + so + vl + ((hf) * 2 + kk) * 4608 + nt * 1024); \
      B[kk * 5 + 4] = *(const LAS bf16x8*)(sm + so + b4 + ((hf) * 2 + kk) * 4608); } } while (0)
#define MM3(B, gi) do { _Pragma("unroll") for (int kk = 0; kk < 2; ++kk) _Pragma("unroll") for (int nt = 0; nt < 5; ++nt) \
      c3[nt] = __builtin_amdgcn_mfma_f32_16x16x32_bf16(B[kk * 5 + nt], G[(gi) + kk], c3[nt], 0, 0, 0); } while (0)
#define ILV3() do { _Pragma("unroll") for (int q_ = 0; q_ < 5; ++q_) { __builtin_amdgcn_sched_group_barrier(0x100, 2, 0); __builtin_amdgcn_sched_group_barrier(0x8, 1, 0); } \
      __builtin_amdgcn_sched_group_barrier(0x8, 5, 0); } while (0)
#define STAGE3(u, LOAD, LAST) do { \
    RD3(B1, 1); MM3(B0, (u) * 4); ILV3(); SCHED(); \
    if (LOAD) { LDG((u) * 4, s * 4 + (u) * 4 + 12); LDG((u) * 4 + 1, s * 4 + (u) * 4 + 13); } \
    if (!(LAST)) { WAITL(0); BAR(); SCHED(); so = so + 18432 == NS3 * 18432 ? 0 : so + 18432; RD3(B0, 0); } \
    MM3(B1, (u) * 4 + 2); if (!(LAST)) ILV3(); SCHED(); \
    if (LOAD) { LDG((u) * 4 + 2, s * 4 + (u) * 4 + 14); LDG((u) * 4 + 3, s * 4 + (u) * 4 + 15); } } while (0)
    RD3(B0, 0);
    int s = 0;
#pragma unroll 1
    for (; s < 15; s += 3) { STAGE3(0, true, false); STAGE3(1, true, false); STAGE3(2, true, false); }
    STAGE3(0, false, false); STAGE3(1, false, false); STAGE3(2, false, true);
#undef STAGE3
#undef LDG
    DST(23);
    u16* op = a.out + (size_t)(tm * 128 + rt * 16 + fr) * D_ + (tn & 7) * 72 + kg * 4;
    LAS const char* hp = sm + T_H + (4 * kg + (fr >> 2)) * 128 + w * 32 + (lane & 3) * 8;
#pragma unroll
    for (int nt = 0; nt < 5; ++nt) {
      const u32x2 h = __builtin_bit_cast(u32x2, trread(hp + nt * 2048));
      const u32x2 o = {cvtpk(c3[nt][0] + bflo(h[0]), c3[nt][1] + bfhi(h[0])), cvtpk(c3[nt][2] + bflo(h[1]), c3[nt][3] + bfhi(h[1]))};
      if (nt < 4 || kg < 2) *(u32x2*)(op + nt * 16) = o;
    }
  }
}

extern "C" void kernel_launch(void* const* d_in, const int* in_sizes, int n_in, void* d_out, int out_size, void* d_ws,
                              size_t ws_size, hipStream_t stream) {
  Args a;
  a.x = (const u16*)d_in[0]; a.are = (const u16*)d_in[1]; a.aim = (const u16*)d_in[2]; a.bre = (const u16*)d_in[3];
  a.bim = (const u16*)d_in[4]; a.cre = (const u16*)d_in[5]; a.cim = (const u16*)d_in[6]; a.logdt = (const u16*)d_in[7];
  a.dskip = (const u16*)d_in[8]; a.win = (const u16*)d_in[10]; a.wsp = (const u16*)d_in[11]; a.bsp = (const u16*)d_in[12];
  a.wout = (const u16*)d_in[13]; a.out = (u16*)d_out; a.ws = (char*)d_ws;
  (void)hipFuncSetAttribute((const void*)fused_block, hipFuncAttributeMaxDynamicSharedMemorySize, LDS_BYTES);
  fused_block<<<256, 512, LDS_BYTES, stream>>>(a);
}
```

```cpp
#include <hip/hip_runtime.h>
#include <hip/hip_bf16.h>

typedef unsigned short u16;
typedef short bf16x8 __attribute__((ext_vector_type(8)));
typedef short s16x4 __attribute__((ext_vector_type(4)));
typedef float f32x4 __attribute__((ext_vector_type(4)));
typedef float f32x2 __attribute__((ext_vector_type(2)));
typedef unsigned u32x2 __attribute__((ext_vector_type(2)));
typedef unsigned u32x4 __attribute__((ext_vector_type(4)));
typedef __bf16 bf2_t __attribute__((ext_vector_type(2)));
typedef __amdgpu_buffer_rsrc_t rsrc_t;
#define LAS __attribute__((address_space(3)))
#define GAS __attribute__((address_space(1)))
typedef GAS unsigned gu32;
typedef GAS unsigned long long gu64;
#define RLX_AGENT __ATOMIC_RELAXED, __HIP_MEMORY_SCOPE_AGENT

constexpr int L_ = 2048, D_ = 576, N_ = 128, FH = 2304, F_ = 4608;
constexpr int TCONV = 24;
constexpr int NXK = 9, NYD = 10 - NXK;
constexpr int GAH = 4;

constexpr size_t WS_EPOCH = 0;
constexpr size_t WS_ST1 = 4096;
constexpr size_t WS_FLAGA = 65536;
constexpr size_t WS_FLAGZ = WS_FLAGA + 65536;
constexpr size_t WS_FLAGG = WS_FLAGZ + 65536;
constexpr size_t WS_SLOT = WS_FLAGG + 65536;
constexpr size_t WS_CS = WS_SLOT + 262144;
constexpr size_t WS_HT = WS_CS + 131072;
constexpr size_t WS_W3T = WS_HT + (size_t)D_ * L_ * 2;
constexpr size_t WS_W1Z = WS_W3T + (size_t)FH * D_ * 2;
constexpr size_t WS_W1Y = WS_W1Z + (size_t)FH * D_ * 2;
constexpr size_t WS_Z2T = WS_W1Y + (size_t)FH * D_ * 2;
constexpr size_t WS_GZ = WS_Z2T + (size_t)L_ * FH * 2;

struct Args {
  const u16 *x, *are, *aim, *bre, *bim, *cre, *cim, *logdt, *dskip, *win, *wsp, *bsp, *wout;
  u16* out;
  char* ws;
};

#define WAITV(n) asm volatile("s_waitcnt vmcnt(%0)" ::"n"(n) : "memory")
#define WAITL(n) asm volatile("s_waitcnt lgkmcnt(%0)" ::"n"(n) : "memory")
#define BAR() do { asm volatile("" ::: "memory"); __builtin_amdgcn_s_barrier(); asm volatile("" ::: "memory"); } while (0)
#define NAP(n) do { __builtin_amdgcn_s_sleep(n); asm volatile("" ::: "memory"); } while (0)
#define LBAR() do { WAITL(0); __builtin_amdgcn_s_barrier(); asm volatile("" ::: "memory"); } while (0)
#define SCHED() __builtin_amdgcn_sched_barrier(0)
constexpr unsigned SPIN_CAP = 1u << 17;
#define CODE_LINES 973

__device__ __forceinline__ unsigned cvtpk(float lo, float hi) {
  const bf2_t r = __builtin_convertvector((f32x2){lo, hi}, bf2_t);
  return __builtin_bit_cast(unsigned, r);
}
__device__ __forceinline__ float bflo(unsigned v) { return __uint_as_float(v << 16); }
__device__ __forceinline__ float bfhi(unsigned v) { return __uint_as_float(v & 0xffff0000u); }
__device__ __forceinline__ float bfr(float x) { return bflo(cvtpk(x, x)); }
__device__ __forceinline__ float bf2f(u16 v) { return __uint_as_float((unsigned)v << 16); }
__device__ __forceinline__ s16x4 trread(LAS const char* p) { return __builtin_amdgcn_ds_read_tr16_b64_v4i16((LAS s16x4*)p); }
__device__ __forceinline__ rsrc_t mkrsrc(const void* p, unsigned bytes) { return __builtin_amdgcn_make_buffer_rsrc((void*)p, 0, bytes, 0x00020000); }
__device__ __forceinline__ void st128(u32x4 v, rsrc_t r, unsigned voff) { __builtin_amdgcn_raw_buffer_store_b128(v, r, voff, 0, 16); }
__device__ __forceinline__ void st64(u32x2 v, rsrc_t r, unsigned voff) { __builtin_amdgcn_raw_buffer_store_b64(v, r, voff, 0, 16); }
__device__ __forceinline__ u32x4 ld128(rsrc_t r, unsigned voff, int soff) { return __builtin_amdgcn_raw_buffer_load_b128(r, voff, soff, 16); }
__device__ __forceinline__ f32x2 ln_scalars(float mean, float var) {
  const float mb = bfr(mean), vb = bfr(fmaxf(var, 0.f));
  return (f32x2){mb, bfr(__builtin_amdgcn_sqrtf(bfr(vb + 1e-5f)))};
}
template <int CTRL> __device__ __forceinline__ float dpp(float v) {
  return __builtin_bit_cast(float, __builtin_amdgcn_update_dpp(0, __builtin_bit_cast(int, v), CTRL, 0xf, 0xf, false));
}
template <int SC1> __device__ __forceinline__ void dma1k(LAS char* dst, rsrc_t r, unsigned voff, int soff) {
  const unsigned m = (unsigned)(size_t)dst;
  if (SC1) asm volatile("s_mov_b32 m0, %0\n\ts_nop 3\n\tbuffer_load_dwordx4 %1, %2, %3 offen sc1 lds" :: "s"(m), "v"(voff), "s"(r), "s"(soff) : "memory");
  else asm volatile("s_mov_b32 m0, %0\n\ts_nop 3\n\tbuffer_load_dwordx4 %1, %2, %3 offen lds" :: "s"(m), "v"(voff), "s"(r), "s"(soff) : "memory");
}
__device__ __forceinline__ void flag_a(char* ws, int idx, unsigned tag, int lane) {
  if (lane < 8) __hip_atomic_store((gu32*)(ws + WS_FLAGA) + lane * 2048 + (idx < 144 ? (idx >> 2) * 32 + (idx & 3) : (27 + (idx >> 4)) * 32 + (idx & 15)), tag, RLX_AGENT);
}

__device__ __forceinline__ unsigned next_tag(unsigned e) { return e * 0x9E3779B1u + 0x7F4A7C15u; }
#define ALLTAG(f) ((f)[0] == tag && (f)[1] == tag && (f)[2] == tag && (f)[3] == tag)
constexpr int STG = 16384 + 18432;
constexpr int T_DEAD = 4 * STG + 3328;
__device__ __forceinline__ void touch(LAS char* sm, rsrc_t r, unsigned voff, int soff) {
  const unsigned m = (unsigned)(size_t)(sm + T_DEAD);
  asm volatile("s_mov_b32 m0, %0\n\ts_nop 3\n\tbuffer_load_dword %1, %2, %3 offen lds" :: "s"(m), "v"(voff), "s"(r), "s"(soff) : "memory");
}

__device__ __forceinline__ int fcol(int tn, int j) { return 576 * (tn >> 2) + (j < 128 ? 128 * (tn & 3) + j : 384 + 16 * (tn & 3) + j); }
__device__ __forceinline__ u32x4 wflags(char* ws, int q) {
  return ld128(mkrsrc(ws + WS_FLAGA + (blockIdx.x & 7) * 8192 + 36 * 128, 18 * 128), (q >> 2) * 128 + (q & 3) * 16, 0);
}
__device__ __forceinline__ u32x4 sflags(char* ws, int lane) { return wflags(ws, min(lane, 26)); }
__device__ __forceinline__ void wait_sflags(char* ws, int lane, unsigned tag) {
  _Pragma("unroll 1") for (unsigned spins = 0; spins < SPIN_CAP; ++spins) {
    const u32x4 f = sflags(ws, lane);
    if (__all(ALLTAG(f))) break;
    NAP(16);
  }
}

__device__ __forceinline__ void ln1_load(const Args& a, int row, u32x2 (&xr)[3]) {
  const int lane = threadIdx.x & 63;
  const u32x2* src = (const u32x2*)(a.x + (size_t)row * D_) + lane;
  xr[0] = src[0]; xr[1] = src[64]; xr[2] = src[lane < 16 ? 128 : 0];
}
__device__ __forceinline__ float wave_sum(float s) {
  s += dpp<0xB1>(s); s += dpp<0x4E>(s); s += dpp<0x141>(s); s += dpp<0x140>(s);
  const int v = __builtin_bit_cast(int, s);
  return (__builtin_bit_cast(float, __builtin_amdgcn_readlane(v, 0)) + __builtin_bit_cast(float, __builtin_amdgcn_readlane(v, 16))) +
         (__builtin_bit_cast(float, __builtin_amdgcn_readlane(v, 32)) + __builtin_bit_cast(float, __builtin_amdgcn_readlane(v, 48)));
}
__device__ __forceinline__ void ln1_publish(const Args& a, int row, const u32x2 (&xr)[3], unsigned tag) {
  const int lane = threadIdx.x & 63;
  f32x2 v[6];
#pragma unroll
  for (int i = 0; i < 6; ++i) v[i] = (f32x2){bflo(xr[i >> 1][i & 1]), bfhi(xr[i >> 1][i & 1])};
  const float m3 = lane < 16 ? 1.f : 0.f;
  f32x2 s = (v[0] + v[1]) + (v[2] + v[3]) + (v[4] + v[5]) * m3;
  const float mean = wave_sum(s.x + s.y) * (1.0f / D_);
#pragma unroll
  for (int i = 0; i < 6; ++i) { v[i] -= mean; v[i] *= v[i]; }
  s = (v[0] + v[1]) + (v[2] + v[3]) + (v[4] + v[5]) * m3;
  const f32x2 sc = ln_scalars(mean, wave_sum(s.x + s.y) * (1.0f / D_));
  if (lane == 0)
    __hip_atomic_store((gu64*)(a.ws + WS_ST1) + row,
                       ((unsigned long long)tag << 32) | (__float_as_uint(sc.y) & 0xffff0000u) | (__float_as_uint(sc.x) >> 16), RLX_AGENT);
}

constexpr int PS_SIDE = 32 * 1056;
__device__ __forceinline__ int slab_off(int row, int col) { return col < 512 ? row * 1056 + col * 2 : PS_SIDE + row * 128 + (col - 512) * 2; }

struct First { u32x2 xr[3]; unsigned ep; u16 are, aim, bre, bim, cre, cim, ld, dsk; };

__device__ __forceinline__ unsigned phase_prep(const Args& a, const First& fi, int p, int wid, LAS char* sm0) {
  char* ws = a.ws;
  const int lane = threadIdx.x & 63, hb = wid >> 2, w = wid & 3;
  const int pg = p / 14, pr = p - pg * 14;
  unsigned tag;
  {
    if (pr < 8) touch(sm0, mkrsrc(a.x, L_ * D_ * 2), (lane >> 1) * (D_ * 2) + (lane & 1) * 140, (pr * 8 + wid) * 32 * (D_ * 2) + pg * 144);
    tag = next_tag(__builtin_amdgcn_readfirstlane(fi.ep));
    ln1_publish(a, blockIdx.x * 8 + wid, fi.xr, tag);
  }
  SCHED(); BAR(); SCHED();
  const int cb0 = p / 3, third = p - 3 * cb0, rl = wid * 8 + (lane >> 3);
  const int g = lane >> 4, fr = lane & 15, q = fr >> 2, pp = lane & 3;
  constexpr int PC = 0, PZ = 2 * 17408, PZS = 192 * 160, PW = PZ + 2 * PZS;
  const bool act = p < 108, act3 = p < 72;
  if (act) {
    const u16* src = a.win + (size_t)(third * 192 + rl) * F_ + cb0 * 64 + (lane & 7) * 8;
    u32x4 v[2][3];
#pragma unroll
    for (int r = 0; r < 2; ++r)
#pragma unroll
      for (int i = 0; i < 3; ++i) v[r][i] = *(const u32x4*)(src + (size_t)i * 64 * F_ + (r ? 0 : FH));
    SCHED();
    if (act3) {
      const int P = p / 9, pi = p - P * 9;
      const rsrc_t rs = mkrsrc(a.wout, FH * D_ * 2);
      const int rb = pi < 8 ? fcol(2 * P + (pi >> 2), 32 * (pi & 3)) : fcol(2 * P, 128);
#pragma unroll
      for (int i = 0; i < 4; ++i) dma1k<0>(sm0 + PW + (4 * wid + i) * 1056, rs, lane * 16, (rb + 4 * wid + i) * (D_ * 2));
      if (wid < 4) dma1k<0>(sm0 + PW + PS_SIDE + wid * 1024, rs, (lane >> 3) * (D_ * 2) + 1024 + (lane & 7) * 16, (rb + 8 * wid) * (D_ * 2));
    }
#pragma unroll
    for (int r = 0; r < 2; ++r) {
      f32x2 s[4] = {};
#pragma unroll
      for (int i = 0; i < 3; ++i) {
        *(LAS u32x4*)(sm0 + PZ + r * PZS + (i * 64 + rl) * 160 + (lane & 7) * 16) = v[r][i];
#pragma unroll
        for (int j = 0; j < 4; ++j) s[j] += (f32x2){bflo(v[r][i][j]), bfhi(v[r][i][j])};
      }
      LAS char* t = sm0 + PC + r * 17408 + rl * 272 + (lane & 7) * 32;
      *(LAS f32x4*)t = (f32x4){s[0].x, s[0].y, s[1].x, s[1].y};
      *(LAS f32x4*)(t + 16) = (f32x4){s[2].x, s[2].y, s[3].x, s[3].y};
    }
  }
  WAITV(0);
  LBAR();

#pragma unroll 1
  for (int round = 0; round < 2; ++round) {
    const int cb = cb0 + (round ? 0 : 36), Q = cb / 9, cq = cb - Q * 9;
    if (act) {
      const int krow = 16 * (g >> 1) + 4 * (g & 1) + q;
      const int tl = 4 * Q + (cq < 8 ? cq >> 1 : w), nt = cq < 8 ? 4 * (cq & 1) + w : 8;
      const rsrc_t rd = mkrsrc(ws + WS_W1Z + (size_t)tl * 165888, 165888);
      const int kst = round ? 1024 : 9216, o0 = lane * 16 + nt * (round ? 18432 : 1024) + (third * 6 + hb * 3) * kst;
      LAS const char* rp = sm0 + PZ + round * PZS + (hb * 96 + krow) * 160 + w * 32 + pp * 8;
#pragma unroll
      for (int ks = 0; ks < 3; ++ks) {
        const s16x4 a0 = trread(rp + ks * 32 * 160), a1 = trread(rp + ks * 32 * 160 + 8 * 160);
        st128(__builtin_bit_cast(u32x4, __builtin_shufflevector(a0, a1, 0, 1, 2, 3, 4, 5, 6, 7)), rd, o0 + ks * kst);
      }
      if (wid == 0) {
        float c = 0.f;
#pragma unroll 16
        for (int r = 0; r < 64; ++r) c += *(LAS const float*)(sm0 + PC + round * 17408 + r * 272 + lane * 4);
        __hip_atomic_store((gu64*)(ws + WS_CS) + third * F_ + cb * 64 + lane, ((unsigned long long)tag << 32) | __float_as_uint(c), RLX_AGENT);
      }
    }
    if (round && act3) {
      const rsrc_t rd = mkrsrc(ws + WS_W3T + ((size_t)wid * 72 + p) * 4608, 4608);
#pragma unroll
      for (int nt = 0; nt < 5; ++nt) {
        const int col = min(wid * 72 + nt * 16 + pp * 4, 572);
        const bool sh = p % 9 == 8;
        const s16x4 a0 = trread(sm0 + PW + slab_off((sh ? 8 : 4) * g + q, col)), a1 = trread(sm0 + PW + slab_off((sh ? 8 : 4) * g + q + (sh ? 4 : 16), col));
        const u32x4 f = __builtin_bit_cast(u32x4, __builtin_shufflevector(a0, a1, 0, 1, 2, 3, 4, 5, 6, 7));
        st128(f, rd, nt < 4 ? nt * 1024 + lane * 16 : (fr < 8 ? 4096 + g * 128 + fr * 16 : 0x10000));
      }
    }
    WAITV(0);
    LBAR();
    if (wid == 1 && act) flag_a(ws, 144 + round * 108 + p, tag, lane);
    if (wid == 2 && round && act3) flag_a(ws, 360 + p, tag, lane);
    BAR();
  }
  if (wid == 0) wait_sflags(ws, lane, tag);
  LBAR();
  return tag;
}

constexpr int CP = 1024, CPS = 8256;
constexpr int CU = CP + 4 * CPS, CUS = 4608;
constexpr int CX = CU + 4 * CUS;
constexpr int CKT = 69632, CKS = 8736;
constexpr int CSC = 0, CSW = 32 * 272;
constexpr int CEX = CKT;


__device__ __forceinline__ int kg_l0(int T, int r) {
  if (T == 0) return 128 * (r >> 3) + (r & 7);
  if (T == 1) return r < 8 ? 256 + 128 * (r >> 2) + 2 * (r & 3) : 512 + 128 * ((r - 8) >> 1) + 4 * (r & 1);
  return 1024 + 128 * r;
}
__device__ __forceinline__ f32x2 cexp_rev(float zr2, float zir, float l) {
  const float m = __builtin_amdgcn_exp2f(zr2 * l);
  const float ph = __builtin_amdgcn_fractf(zir * l);
  return (f32x2){m * __builtin_amdgcn_cosf(ph), m * __builtin_amdgcn_sinf(ph)};
}
__device__ __forceinline__ f32x2 cmul(f32x2 a, f32x2 b) {
  f32x2 t, r;
  asm("v_pk_mul_f32 %0, %1, %2 op_sel_hi:[0,1]" : "=v"(t) : "v"(a), "v"(b));
  asm("v_pk_fma_f32 %0, %1, %2, %3 op_sel:[1,1,0] op_sel_hi:[1,0,1] neg_lo:[0,1,0]" : "=v"(r) : "v"(a), "v"(b), "v"(t));
  return r;
}
typedef _Float16 h2_t __attribute__((ext_vector_type(2)));
typedef _Float16 f16x8 __attribute__((ext_vector_type(8)));
__device__ __forceinline__ void put_h(LAS char* p, f32x2 v) { *(LAS h2_t*)p = __builtin_convertvector(v, h2_t); }
__device__ __forceinline__ float wave_max(float s) {
  s = fmaxf(s, dpp<0xB1>(s)); s = fmaxf(s, dpp<0x4E>(s)); s = fmaxf(s, dpp<0x141>(s)); s = fmaxf(s, dpp<0x140>(s));
  const int v = __builtin_bit_cast(int, s);
  return fmaxf(fmaxf(__builtin_bit_cast(float, __builtin_amdgcn_readlane(v, 0)), __builtin_bit_cast(float, __builtin_amdgcn_readlane(v, 16))),
               fmaxf(__builtin_bit_cast(float, __builtin_amdgcn_readlane(v, 32)), __builtin_bit_cast(float, __builtin_amdgcn_readlane(v, 48))));
}
#define WFENCE() do { asm volatile("" ::: "memory"); __builtin_amdgcn_wave_barrier(); asm volatile("" ::: "memory"); } while (0)

__device__ __forceinline__ unsigned phase_conv(const Args& a, const First& fi, int cgp, int wid, LAS char* sm) {
  char* ws = a.ws;
  const int tid = threadIdx.x, lane = tid & 63, d0 = cgp * 4;
  const u16 vare = fi.are, vaim = fi.aim, vbre = fi.bre, vbim = fi.bim, vcre = fi.cre, vcim = fi.cim, vld = fi.ld, vdsk = fi.dsk;
  u32x2 xg[4];
  const rsrc_t rx = mkrsrc(a.x + d0, L_ * D_ * 2 - d0 * 2);
#define XG(i) do { SCHED(); xg[i] = __builtin_amdgcn_raw_buffer_load_b64(rx, tid * (4 * D_ * 2), ((cgp + (i)) & 3) * (D_ * 2), 0); SCHED(); } while (0)
  SCHED();
  const int r = lane & 15, kg = lane >> 4;
  int kto[3]; float lf[3];
#pragma unroll
  for (int T = 0; T < 3; ++T) {
    const int l0 = kg_l0(T, kg * 4);
    kto[T] = CKT + wid * CKS + 16 + (8 * r + (r >> 1) + l0 + (l0 >> 4)) * 4; lf[T] = (float)(l0 + 8 * r);
    asm volatile("" : "+v"(kto[T]), "+v"(lf[T]));
  }
  SCHED();
  const unsigned tag = next_tag(__builtin_amdgcn_readfirstlane(fi.ep));
  ln1_publish(a, blockIdx.x * 8 + wid, fi.xr, tag);
  XG(0);
  {
    const float dt = bfr(__expf(bf2f(vld)));
    float zr = dt * bf2f(vare) * 1.44269504f;
    const float zi = dt * bf2f(vaim) * 0.15915494f;
    const float br = dt * bf2f(vbre), bi = dt * bf2f(vbim), cr = bf2f(vcre), ci = bf2f(vcim);
    f32x2 f = {cr * br - ci * bi, -(cr * bi + ci * br)};
    const float zmax = wave_max(zr);
    const int ew = __builtin_amdgcn_frexp_expf(wave_max(fmaxf(fabsf(f.x), fabsf(f.y))));
    zr -= zmax;
    f.x = __builtin_amdgcn_ldexpf(f.x, -ew); f.y = __builtin_amdgcn_ldexpf(f.y, -ew);
    LAS char* wr = sm + CSC + wid * CSW + lane * 4;
    LAS const char* rd = sm + CSC + wid * CSW + r * 272 + kg * 16;
    {
      const f32x2 q = cexp_rev(zr, -zi, 8.f), q2 = cmul(q, q);
      f32x2 f1 = cmul(f, q);
#pragma unroll
      for (int b = 0; b < 16; b += 2) { put_h(wr + (16 + b) * 272, f); put_h(wr + (17 + b) * 272, f1); if (b < 14) { f = cmul(f, q2); f1 = cmul(f1, q2); } }
    }
    XG(1);
    f32x2 e[8], g[8];
    e[0] = (f32x2){1.f, 0.f}; e[1] = cexp_rev(zr, zi, 1.f);
#pragma unroll
    for (int i = 2; i < 8; ++i) e[i] = cmul(e[i >> 1], e[i - (i >> 1)]);
    g[1] = cexp_rev(zr, zi, 128.f);
#pragma unroll
    for (int i = 2; i < 8; ++i) g[i] = cmul(g[i >> 1], g[i - (i >> 1)]);
    f16x8 B[4];
    const float fe = (float)ew, e1 = __builtin_amdgcn_exp2f(zmax), e2 = e1 * e1, e4 = e2 * e2, g1s = __builtin_amdgcn_exp2f(zmax * 128.f);
    const float es[4] = {1.f, e1, e2, e2 * e1}, es2[4] = {1.f, e2, e4, e4 * e2}, es4[4] = {1.f, e4, g1s, g1s * e4}, gs[4] = {1.f, g1s, g1s * g1s, g1s * g1s * g1s};
#pragma unroll
    for (int T = 0; T < 3; ++T) {
      LAS char* wt = wr + (T & 1) * 16 * 272;
      if (T == 0) {
#pragma unroll
        for (int i = 0; i < 8; ++i) { put_h(wt + i * 272, e[i]); put_h(wt + (8 + i) * 272, i ? cmul(g[1], e[i]) : g[1]); }
        WFENCE();
#pragma unroll
        for (int st = 0; st < 4; ++st) B[st] = *(LAS const f16x8*)(rd + 16 * 272 + st * 64);
      } else if (T == 1) {
#pragma unroll
        for (int i = 0; i < 8; ++i) {
          put_h(wt + i * 272, (i & 3) ? cmul(g[2 + (i >> 2)], e[2 * (i & 3)]) : g[2 + (i >> 2)]);
          put_h(wt + (8 + i) * 272, (i & 1) ? cmul(g[4 + (i >> 1)], e[4]) : g[4 + (i >> 1)]);
        }
      } else {
        const f32x2 g8 = cmul(g[4], g[4]);
        put_h(wt, g8);
#pragma unroll
        for (int i = 1; i < 8; ++i) put_h(wt + i * 272, cmul(g8, g[i]));
        put_h(wt + 8 * 272, cmul(g8, g8));
      }
      WFENCE();
      if (T < 2) XG(T + 2);
      f32x4 acc = {};
#pragma unroll
      for (int st = 0; st < 4; ++st)
        acc = __builtin_amdgcn_mfma_f32_16x16x32_f16(*(LAS const f16x8*)(rd + (T & 1) * 16 * 272 + st * 64), B[st], acc, 0, 0, 0);
      WFENCE();
      const float s0 = __builtin_amdgcn_exp2f(__builtin_fmaf(zmax, lf[T], fe));
#pragma unroll
      for (int j = 0; j < 4; ++j) {
        const float sj = T == 0 ? es[j] : T == 2 ? gs[j] : (kg < 2 ? es2[j] : es4[j]);
        const int oj = T == 0 ? 4 * j : T == 2 ? 544 * j : (kg < 2 ? 8 * j : 16 * (j & 1) + 544 * (j >> 1));
        if (T < 2 || kg < 2 || (lane == 32 && j == 0)) *(LAS float*)(sm + kto[T] + oj) = acc[j] * (j ? s0 * sj : s0);
      }
    }
  }
  const rsrc_t rst = mkrsrc(ws + WS_ST1, 16384);
  u32x4 g0 = ld128(rst, tid * 32, 0), g1 = ld128(rst, tid * 32 + 16, 0);
  LBAR();
  {
#define KS(i) (k[(i) + ((i) >> 4)] + k[(i) + ((i) >> 4) + CKS / 4])
    float kv[17];
    int ch, T0;
    if (wid == 0) {
      ch = lane >> 4; T0 = r * 16;
      LAS const float* k = (LAS const float*)(sm + CKT + 2 * ch * CKS + 16) + T0 + (T0 >> 4);
#pragma unroll
      for (int i = 0; i < 17; ++i) kv[i] = KS(i - 1);
      if (r == 0) kv[0] = 0.f;
    } else if (wid == 1) {
      ch = lane >> 4; T0 = 256 + r * 16;
      LAS const float* k = (LAS const float*)(sm + CKT + 2 * ch * CKS + 16) + T0 + (T0 >> 4);
#pragma unroll
      for (int i = 0; i < 16; ++i) kv[i + 1] = KS((i + ((i >> 1) & 1)) & ~1);
      kv[0] = KS(r == 0 ? -1 : 0);
    } else if (wid < 4) {
      ch = (tid - 128) >> 5; T0 = 512 + (lane & 31) * 16;
      LAS const float* k = (LAS const float*)(sm + CKT + 2 * ch * CKS + 16) + T0 + (T0 >> 4);
#pragma unroll
      for (int i = 0; i < 16; ++i) kv[i + 1] = KS((i + 1 + ((i >> 2) & 1)) & ~3);
      kv[0] = KS(0);
    } else {
      ch = wid - 4; T0 = 1024 + lane * 16;
      LAS const float* k = (LAS const float*)(sm + CKT + 2 * ch * CKS + 16) + T0 + (T0 >> 4);
#pragma unroll
      for (int i = 0; i < 16; ++i) kv[i + 1] = KS((i + 3 + ((i >> 3) & 1)) & ~7);
      kv[0] = KS(0);
    }
    u32x4 o[4];
#pragma unroll
    for (int c = 0; c < 4; ++c)
#pragma unroll
      for (int i = 0; i < 4; ++i) { const int j = 15 - 4 * c - i; o[c][i] = cvtpk(kv[j + 1], kv[j]); }
    LAS char* pw = sm + CP + ch * CPS + (2032 - T0) * 4;
#pragma unroll
    for (int c = 0; c < 4; ++c) {
      asm volatile("" : "+v"(o[c]));
    }
    const bool sw = lane & 2;
#pragma unroll
    for (int c = 0; c < 4; ++c) {
      u32x4 v;
#pragma unroll
      for (int i = 0; i < 4; ++i) { const unsigned x0 = o[c][i], x1 = o[c ^ 2][i]; v[i] = sw ? x1 : x0; }
      *(LAS u32x4*)(pw + (c < 2 ? 1 : -1) * (lane & 2) * 16 + c * 16) = v;
    }
    if (tid < 64) *(LAS unsigned*)(sm + CP + (tid >> 4) * CPS + 8192 + (tid & 15) * 4) = 0u;
    *(LAS unsigned*)(sm + CU + (tid >> 7) * CUS + ((tid >> 6) & 1) * 2304 + (tid & 63) * 4) = 0u;
  }
  _Pragma("unroll 1") for (unsigned spins = 0; spins < SPIN_CAP; ++spins) {
    if (__all(g0[1] == tag && g0[3] == tag && g1[1] == tag && g1[3] == tag)) break;
    NAP(8);
    g0 = ld128(rst, tid * 32, 0); g1 = ld128(rst, tid * 32 + 16, 0);
  }
  {
    if (cgp & 1) { const u32x2 t = xg[3]; xg[3] = xg[2]; xg[2] = xg[1]; xg[1] = xg[0]; xg[0] = t; }
    if (cgp & 2) { const u32x2 t = xg[0], v = xg[1]; xg[0] = xg[2]; xg[1] = xg[3]; xg[2] = t; xg[3] = v; }
    const unsigned sv[4] = {g0[0], g0[2], g1[0], g1[2]};
    unsigned uu[4][2];
#pragma unroll
    for (int i = 0; i < 4; ++i) {
      const float m = bflo(sv[i]), rs = __builtin_amdgcn_rcpf(bfhi(sv[i]));
#pragma unroll
      for (int j = 0; j < 2; ++j) {
        const unsigned dd = cvtpk(bflo(xg[i][j]) - m, bfhi(xg[i][j]) - m);
        uu[i][j] = cvtpk(bflo(dd) * rs, bfhi(dd) * rs);
      }
    }
#pragma unroll
    for (int c = 0; c < 4; ++c) {
      const unsigned sel = (c & 1) ? 0x07060302u : 0x05040100u;
      const u32x2 pu = {__builtin_amdgcn_perm(uu[1][c >> 1], uu[0][c >> 1], sel), __builtin_amdgcn_perm(uu[3][c >> 1], uu[2][c >> 1], sel)};
      const u32x2 px = {__builtin_amdgcn_perm(xg[1][c >> 1], xg[0][c >> 1], sel), __builtin_amdgcn_perm(xg[3][c >> 1], xg[2][c >> 1], sel)};
      *(LAS u32x2*)(sm + CU + c * CUS + 256 + ((tid >> 1) & 1) * 2304 + (tid >> 2) * 16 + (tid & 1) * 8) = pu;
      *(LAS u32x2*)(sm + CX + c * 4096 + tid * 8) = px;
    }
  }
  LBAR();
  touch(sm, mkrsrc(a.wsp + (size_t)(cgp * 8 + wid) * TCONV * 64, TCONV * 128), lane * 128, 0);
  const int ch = wid & 3, hf = wid >> 2, n = r;
  f32x4 acc[8] = {}, res[4];
  {
    LAS const char* pa = sm + CP + ch * CPS + 4 * (2047 - n - 16 * (kg & 1) + 8 * (kg >> 1) - 32 * 63) - hf * 512;
    LAS const char* pb = sm + CU + ch * CUS + 256 + (kg >> 1) * 2304 + 16 * (n - (kg & 1)) - hf * 128;
    bf16x8 A0[8], B0[8], A1[8], B1[8];
#define CRD(A, B, rr) do { _Pragma("unroll") for (int c = 0; c < 8; ++c) { \
      LAS const unsigned* q_ = (LAS const unsigned*)(pa + (63 - 8 * c - (rr)) * 128); \
      A[c] = __builtin_bit_cast(bf16x8, (u32x4){q_[0], q_[2], q_[4], q_[6]}); \
      B[c] = *(LAS const bf16x8*)(pb + (8 * c - (rr)) * 32); } } while (0)
#define CMM(A, B) do { _Pragma("unroll") for (int m = 0; m < 8; ++m) { \
      _Pragma("unroll") for (int c = 0; c <= m; ++c) if (c + m < 8) acc[c + m] = __builtin_amdgcn_mfma_f32_16x16x32_bf16(A[c], B[m], acc[c + m], 0, 0, 0); \
      _Pragma("unroll") for (int c = 0; c < m; ++c) if (c + m < 8) acc[c + m] = __builtin_amdgcn_mfma_f32_16x16x32_bf16(A[m], B[c], acc[c + m], 0, 0, 0); } } while (0)
#define CILV() do { _Pragma("unroll") for (int q_ = 0; q_ < 24; ++q_) { __builtin_amdgcn_sched_group_barrier(0x100, 1, 0); __builtin_amdgcn_sched_group_barrier(0x8, 1, 0); } \
      __builtin_amdgcn_sched_group_barrier(0x8, 12, 0); } while (0)
    CRD(A0, B0, 0); SCHED();
    CRD(A1, B1, 1); CMM(A0, B0); CILV(); SCHED();
    CRD(A0, B0, 2); CMM(A1, B1); CILV(); SCHED();
    CRD(A1, B1, 3); CMM(A0, B0); CILV(); SCHED();
    const float dsk = bf2f(vdsk);
    LAS const char* pu = sm + CU + ch * CUS + 256 + (kg >> 1) * 2304 + hf * 1024 + 16 * n + 8 * (kg & 1);
    LAS const char* px = sm + CX + ch * 4096 + hf * 2048 + n * 32 + kg * 8;
#pragma unroll
    for (int t = 0; t < 4; ++t) {
      const u32x2 uv = *(LAS const u32x2*)(pu + t * 256), xv = *(LAS const u32x2*)(px + t * 512);
#pragma unroll
      for (int j = 0; j < 2; ++j) {
        float r0 = __builtin_fmaf(bflo(uv[j]), dsk, bflo(xv[j])), r1 = __builtin_fmaf(bfhi(uv[j]), dsk, bfhi(xv[j]));
        asm("" : "+v"(r0)); asm("" : "+v"(r1));
        res[t][2 * j] = r0; res[t][2 * j + 1] = r1;
      }
    }
    CMM(A1, B1);
    __builtin_amdgcn_sched_group_barrier(0x100, 8, 0);
    __builtin_amdgcn_sched_group_barrier(0x8, 6, 0);
#pragma unroll
    for (int q_ = 0; q_ < 26; ++q_) { __builtin_amdgcn_sched_group_barrier(0x2, 2, 0); __builtin_amdgcn_sched_group_barrier(0x8, 1, 0); }
    SCHED();
  }
#pragma unroll
  for (int t = 0; t < 4; ++t) *(LAS f32x4*)(sm + CEX + ((wid * 4 + t) * 64 + lane) * 16) = hf ? acc[t] : acc[4 + t];
  LBAR();
  {
    const int d = d0 + ch;
    const rsrc_t rh = mkrsrc(ws + WS_HT, D_ * L_ * 2);
    u32x2 o[4];
#pragma unroll
    for (int t = 0; t < 4; ++t) {
      const f32x4 h4 = ((hf ? acc[4 + t] : acc[t]) + res[t]) + *(LAS const f32x4*)(sm + CEX + (((wid ^ 4) * 4 + t) * 64 + lane) * 16);
      o[t] = (u32x2){cvtpk(h4[0], h4[1]), cvtpk(h4[2], h4[3])};
    }
    const unsigned ho = ((hf * 8 + 2 * (kg & 1) + (n >> 3)) * D_ + d) * 256 + (((n ^ d) & 7) << 5) + (kg >> 1) * 16;
#pragma unroll
    for (int t = 0; t < 4; t += 2) {
      const auto s0 = __builtin_amdgcn_permlane16_swap(o[t][0], o[t + 1][0], false, false);
      const auto s1 = __builtin_amdgcn_permlane16_swap(o[t][1], o[t + 1][1], false, false);
      st128((u32x4){s0[0], s1[0], s0[1], s1[1]}, rh, ho + t * 2 * D_ * 256);
    }
  }
  SCHED();
  const u32x4 fw = sflags(ws, lane);
  WAITV(1);
  LBAR();
  if (wid == 3) flag_a(ws, cgp, tag, lane);
  if (!__all(ALLTAG(fw))) wait_sflags(ws, lane, tag);
  return tag;
}

constexpr int RING = 4 * STG;
constexpr int T_CS = RING, T_LN2 = RING + 1152, T_LN3 = RING + 2176, T_PROG = RING + 3200, T_E = RING + 3584, T_H = 7 * 18432;
constexpr int LDS_BYTES = RING + 3584 + 18944;
constexpr unsigned CODE_BYTES = CODE_LINES * 64;
__device__ __forceinline__ constexpr int so3(int s) { return (s & 7) == 7 ? T_E : (((s & 7) + 4) % 7) * 18432; }

template <int SC1> __device__ __forceinline__ void issue_a(LAS char* dst, int w, rsrc_t ra, unsigned va, int sa, int pa) {
#pragma unroll
  for (int i = 0; i < 4; ++i) dma1k<SC1>(dst + (4 * i + w) * 1024, ra, va, sa + (4 * i + w) * pa);
}
__device__ __forceinline__ void issue_b(LAS char* dst, int w, rsrc_t rb, unsigned vb, int sb) {
#pragma unroll
  for (int i = 0; i < 4; ++i) dma1k<1>(dst + (4 * i + w) * 1024, rb, vb, sb + (4 * i + w) * 1024);
  if (w < 2) dma1k<1>(dst + (16 + w) * 1024, rb, vb, sb + (16 + w) * 1024);
}
template <int NA, int NB, int NX = 0> __device__ __forceinline__ void wait_parts(int w) {
  if (w < 2) WAITV(NA * 4 + NB * 5 + NX); else WAITV(NA * 4 + NB * 4 + NX);
}
constexpr unsigned LDS_SPIN_CAP = 1u << 23;
__device__ __forceinline__ void ysync_wait(LAS char* sm, unsigned k) {
  _Pragma("unroll 1") for (unsigned spins = 0; spins < LDS_SPIN_CAP; ++spins) {
    const u32x4 v = *(volatile LAS u32x4*)(sm + T_PROG);
    if (__builtin_amdgcn_readfirstlane(min(min(v[0], v[1]), min(v[2], v[3]))) >= k) break;
    NAP(1);
  }
  asm volatile("" ::: "memory");
}
__device__ __forceinline__ f32x2 gelu2(f32x2 x) {
  const f32x2 a = x * ((x * x) * (-2.3022082f * 0.044715f) + (-2.3022082f));
  f32x2 e; e.x = __builtin_amdgcn_exp2f(a.x); e.y = __builtin_amdgcn_exp2f(a.y);
  e = e + 1.0f;
  f32x2 r; r.x = __builtin_amdgcn_rcpf(e.x); r.y = __builtin_amdgcn_rcpf(e.y);
  return x * r;
}

#define MMX(F) do { _Pragma("unroll") for (int nt = 0; nt < 9; ++nt) _Pragma("unroll") for (int mt = 0; mt < 2; ++mt) \
    acc[mt][nt] = __builtin_amdgcn_mfma_f32_16x16x32_bf16(F[mt], F[2 + nt], acc[mt][nt], 0, 0, 0); } while (0)
#define MMY(F) do { _Pragma("unroll") for (int nt = 0; nt < 9; ++nt) _Pragma("unroll") for (int mt = 0; mt < 2; ++mt) \
    acc[mt][nt] = __builtin_amdgcn_mfma_f32_16x16x32_bf16(F[2 + nt], F[mt], acc[mt][nt], 0, 0, 0); } while (0)
#define RDB(F, so, ks) do { _Pragma("unroll") for (int nt = 0; nt < 9; ++nt) \
    F[2 + nt] = *(const LAS bf16x8*)(sm + (so) + boff + ((ks) * 9 + nt) * 1024); } while (0)
#define RDA(F, so, ks) do { _Pragma("unroll") for (int mt = 0; mt < 2; ++mt) \
    F[mt] = *(const LAS bf16x8*)(sm + (so) + (aoff2 ^ ((ks) * 64)) + mt * 2048); } while (0)
#define RDAT(F, so, ks, MTX) do { _Pragma("unroll") for (int mt = 0; mt < 2; ++mt) { \
    const s16x4 t0_ = trread(sm + (so) + (aoff ^ (mt * (MTX))) + (ks) * 8192), t1_ = trread(sm + (so) + (aoff ^ (mt * (MTX))) + (ks) * 8192 + 2048); \
    F[mt] = __builtin_shufflevector(t0_, t1_, 0, 1, 2, 3, 4, 5, 6, 7); } } while (0)
#define ILV2(NR2, NR1, NM) do { _Pragma("unroll") for (int q_ = 0; q_ < (NR2); ++q_) { __builtin_amdgcn_sched_group_barrier(0x100, 2, 0); __builtin_amdgcn_sched_group_barrier(0x8, 1, 0); } \
    _Pragma("unroll") for (int q_ = 0; q_ < (NR1); ++q_) { __builtin_amdgcn_sched_group_barrier(0x100, 1, 0); __builtin_amdgcn_sched_group_barrier(0x8, 1, 0); } \
    __builtin_amdgcn_sched_group_barrier(0x8, (NM) - (NR2) - (NR1), 0); } while (0)
#define ILV(NR) do { _Pragma("unroll") for (int q_ = 0; q_ < (NR); ++q_) { __builtin_amdgcn_sched_group_barrier(0x100, 1, 0); __builtin_amdgcn_sched_group_barrier(0x8, 1, 0); } \
    __builtin_amdgcn_sched_group_barrier(0x8, 18 - (NR), 0); } while (0)
#define ZERO_ACC() do { _Pragma("unroll") for (int mt = 0; mt < 2; ++mt) _Pragma("unroll") for (int nt = 0; nt < 9; ++nt) acc[mt][nt] = (f32x4){0.f, 0.f, 0.f, 0.f}; } while (0)

__global__ __launch_bounds__(512, 2) void fused_block(Args a) {
  extern __shared__ __attribute__((aligned(16))) char smem[];
  LAS char* sm = (LAS char*)smem;
  char* ws = a.ws;
  const int wid = __builtin_amdgcn_readfirstlane(threadIdx.x >> 6), w = wid & 3;
  const int xcd = blockIdx.x & 7, idx = blockIdx.x >> 3;

  unsigned long long pc;
  asm volatile("s_getpc_b64 %0" : "=s"(pc));
#define TOUCH_CODE() touch(sm, mkrsrc((const void*)pc, CODE_BYTES), (threadIdx.x & 63) * 64, (idx & 15) * 4096)
  if (wid == 7) TOUCH_CODE();
  if (wid == 6) touch(sm, mkrsrc(ws + WS_FLAGA + blockIdx.x * 768, 768), (threadIdx.x & 63) * 128, 0);
  if (wid < 4) {
    const int b = blockIdx.x, tm0 = (b & 1) * 8 + ((b >> 3) & 7), tn0 = ((b >> 1) & 3) * 4 + (b >> 6);
    st128((u32x4){~0u, ~0u, ~0u, ~0u}, mkrsrc(ws + WS_SLOT + (size_t)((tm0 * 4 + wid) * 16 + tn0) * 256, 256), (threadIdx.x & 63) * 16);
  }
  SCHED();
  First fi;
  {
    const int d0 = (xcd * 18 + min(idx, 17)) * 4, pi = d0 * N_ + threadIdx.x;
    ln1_load(a, blockIdx.x * 8 + wid, fi.xr);
    fi.ep = __hip_atomic_load((gu32*)(ws + WS_EPOCH), RLX_AGENT);
    fi.are = a.are[pi]; fi.aim = a.aim[pi]; fi.bre = a.bre[pi]; fi.bim = a.bim[pi]; fi.cre = a.cre[pi]; fi.cim = a.cim[pi];
    fi.ld = a.logdt[d0 + (threadIdx.x >> 7)]; fi.dsk = a.dskip[d0 + w];
    asm volatile("" ::: "memory");
  }
  SCHED();
  const unsigned tag = __builtin_amdgcn_readfirstlane(__builtin_expect(idx < 18, 1) ? phase_conv(a, fi, xcd * 18 + idx, wid, sm)
                                                                               : phase_prep(a, fi, xcd * 14 + idx - 18, wid, sm));
  int bb = blockIdx.x;
  asm volatile("" : "+s"(bb));
  const int tm = (bb & 1) * 8 + ((bb >> 3) & 7), tn = ((bb >> 1) & 3) * 4 + (bb >> 6);
  const int t3 = 16 * (tm >> 3) + tn, tc = tm & 7;
  const rsrc_t rh = mkrsrc(ws + WS_HT + (size_t)tm * D_ * 256, D_ * 256);
#define LANE_CONSTS() int lane = threadIdx.x & 63; asm volatile("" : "+v"(lane)); \
  const int fr = lane & 15, kg = lane >> 4; const unsigned vl = lane * 16; \
  const int krow = 16 * (kg >> 1) + 4 * (kg & 1) + (fr >> 2), boff = 16384 + lane * 16
#define ISS1A(s) issue_a<1>(sm + ((s) & 3) * STG, w, rh, vl, (s) * 16384, 1024)
#define ISS1B(s) issue_b(sm + ((s) & 3) * STG + 16384, w, rw1, vl, (s) * 18432)
#define ISS1(s) do { ISS1A(s); ISS1B(s); } while (0)
  const rsrc_t rs = mkrsrc(a.wsp + (size_t)tm * 128 * L_, 128 * L_ * 2), rz = mkrsrc(ws + WS_Z2T + (size_t)tn * 32 * 18432, 32 * 18432);
#define ISS2A(g) issue_a<0>(sm + (((g) + 2) & 3) * STG, w, rs, vs, (g) * 128, 8 * 4096)
#define ISS2B(g) issue_b(sm + (((g) + 2) & 3) * STG + 16384, w, rz, vl, (g) * 18432)
  f32x4 acc[2][9];
  bf16x8 F0[11], F1[11];
  if (wid < 4) {
    LANE_CONSTS();
    const int q = fr >> 2, p = lane & 3;
    const int aoff = krow * 256 + (((2 * w + (p >> 1)) ^ (krow & 7)) << 5) + (p & 1) * 16;
    f32x4 sum[2] = {}, sq[2] = {};
    const bf16x8 ones = {0x3f80, 0x3f80, 0x3f80, 0x3f80, 0x3f80, 0x3f80, 0x3f80, 0x3f80};
#define STATS(F) do { _Pragma("unroll") for (int mt = 0; mt < 2; ++mt) { \
      sum[mt] = __builtin_amdgcn_mfma_f32_16x16x32_bf16(F[mt], ones, sum[mt], 0, 0, 0); sq[mt] = __builtin_amdgcn_mfma_f32_16x16x32_bf16(F[mt], F[mt], sq[mt], 0, 0, 0); } } while (0)
#define ILVP() ILV2(0, 13, 22)
#define ITER(s) do { \
    RDAT(F1, ((s) & 3) * STG, 1, 8); RDB(F1, ((s) & 3) * STG, 1); MMX(F0); STATS(F0); ILVP(); SCHED(); \
    WAITL(0); BAR(); SCHED(); \
    if ((s) < 8) { RDAT(F0, (((s) + 1) & 3) * STG, 0, 8); RDB(F0, (((s) + 1) & 3) * STG, 0); MMX(F1); STATS(F1); ILVP(); } else { STATS(F1); SCHED(); MMX(F1); } \
    if ((s) < 8) SCHED(); } while (0)
    if (w == 3) {
      const rsrc_t rf = mkrsrc(ws + WS_FLAGA + (bb & 7) * 8192, 36 * 128);
      SCHED(); BAR(); SCHED();
      if (idx >= 18) {
        _Pragma("unroll 1") for (unsigned spins = 0; spins < SPIN_CAP; ++spins) {
          const u32x4 f = ld128(rf, (idx - 18) * 256, 0);
          if (__all(ALLTAG(f))) break;
          NAP(24);
        }
      } else NAP(12);
      _Pragma("unroll 1") for (unsigned spins = 0; spins < SPIN_CAP; ++spins) {
        const u32x4 f = ld128(rf, min(lane, 35) * 128, 0);
        if (__all(ALLTAG(f))) break;
        NAP(6);
      }
    } else {
      BAR();
      const int t = w * 64 + lane;
      const rsrc_t rc = mkrsrc(ws + WS_CS, 3 * F_ * 8);
      const unsigned vo = (t < 72 ? FH + fcol(tn, t * 2) : fcol(tn, min(t - 72, 71) * 2)) * 8;
      u32x4 c0, c1, c2;
      _Pragma("unroll 1") for (unsigned spins = 0; spins < SPIN_CAP; ++spins) {
        c0 = ld128(rc, vo, 0); c1 = ld128(rc, vo, F_ * 8); c2 = ld128(rc, vo, 2 * F_ * 8);
        if (__all(c0[1] == tag && c0[3] == tag && c1[1] == tag && c1[3] == tag && c2[1] == tag && c2[3] == tag)) break;
        NAP(16);
      }
      if (t < 144) *(LAS f32x2*)(sm + T_CS + t * 8) = (f32x2){__uint_as_float(c0[0]) + __uint_as_float(c1[0]) + __uint_as_float(c2[0]),
                                                               __uint_as_float(c0[2]) + __uint_as_float(c1[2]) + __uint_as_float(c2[2])};
      if (t < 8) *(LAS unsigned*)(sm + T_PROG + t * 4) = 0u;
    }
    ZERO_ACC();
    LBAR();
    BAR();
    RDAT(F0, 0, 0, 8); RDB(F0, 0, 0);
    ITER(0); ITER(1); ITER(2); ITER(3); ITER(4); ITER(5); ITER(6); ITER(7);
    __builtin_amdgcn_s_setprio(2);
    ITER(8);
#undef ITER
#undef STATS
    f32x4 rs4[2], nm4[2], s1[2] = {}, s2[2] = {};
#pragma unroll
    for (int mt = 0; mt < 2; ++mt) {
      const float qv = (fr & 2) ? ((fr & 1) ? sq[mt][3] : sq[mt][2]) : ((fr & 1) ? sq[mt][1] : sq[mt][0]);
      const float sv = (fr & 2) ? ((fr & 1) ? sum[mt][3] : sum[mt][2]) : ((fr & 1) ? sum[mt][1] : sum[mt][0]);
      const float mean = sv * (1.0f / D_);
      const f32x2 sc = ln_scalars(mean, qv * (1.0f / D_) - mean * mean);
      const float ri = __builtin_amdgcn_rcpf(sc.y);
      *(LAS f32x2*)(sm + (kg == q ? T_LN2 + (w * 32 + 8 * kg + 4 * mt + p) * 8 : T_DEAD + fr * 8)) = (f32x2){ri, -sc.x * ri};
    }
    __builtin_amdgcn_sched_group_barrier(0x8, 2, 0);
#pragma unroll
    for (int q_ = 0; q_ < 16; ++q_) { __builtin_amdgcn_sched_group_barrier(0x402, 4, 0); __builtin_amdgcn_sched_group_barrier(0x8, 1, 0); }
    WFENCE();

#pragma unroll
    for (int mt = 0; mt < 2; ++mt) {
      const f32x4 t0 = *(const LAS f32x4*)(sm + T_LN2 + (w * 32 + 8 * kg + 4 * mt) * 8), t1 = *(const LAS f32x4*)(sm + T_LN2 + (w * 32 + 8 * kg + 4 * mt) * 8 + 16);
      rs4[mt] = (f32x4){t0[0], t0[2], t1[0], t1[2]}; nm4[mt] = (f32x4){t0[1], t0[3], t1[1], t1[3]};
    }
#pragma unroll
    for (int nt = 0; nt < 9; ++nt) {
      const float c = *(const LAS float*)(sm + T_CS + (nt * 16 + fr) * 4);
#pragma unroll
      for (int mt = 0; mt < 2; ++mt) {
        const f32x4 v = acc[mt][nt] * rs4[mt] + nm4[mt] * c;
        const f32x2 g0 = gelu2((f32x2){v[0], v[1]}), g1 = gelu2((f32x2){v[2], v[3]});
        const f32x4 z = {g0.x, g0.y, g1.x, g1.y};
        acc[mt][nt] = z; s1[mt] += z; s2[mt] += z * z;
      }
    }
    const rsrc_t rsl = mkrsrc(ws + WS_SLOT + (size_t)(tm * 4 + w) * 4096, 4096);
    {
      float v8[2][8], v4[2][4], v2[2][2], v1[2];
#pragma unroll
      for (int i = 0; i < 8; ++i) { v8[0][i] = s1[i >> 2][i & 3]; v8[1][i] = s2[i >> 2][i & 3]; }
#pragma unroll
      for (int u = 0; u < 2; ++u) {
        const bool b2 = fr & 4, b1 = fr & 2, b0 = fr & 1;
#pragma unroll
        for (int i = 0; i < 4; ++i) { const float k = b2 ? v8[u][4 + i] : v8[u][i], sn = b2 ? v8[u][i] : v8[u][4 + i]; v4[u][i] = k + dpp<0x141>(dpp<0x1B>(sn)); }
#pragma unroll
        for (int i = 0; i < 2; ++i) { const float k = b1 ? v4[u][2 + i] : v4[u][i], sn = b1 ? v4[u][i] : v4[u][2 + i]; v2[u][i] = k + dpp<0x4E>(sn); }
        { const float k = b0 ? v2[u][1] : v2[u][0], sn = b0 ? v2[u][0] : v2[u][1]; v1[u] = k + dpp<0xB1>(sn); }
        v1[u] += dpp<0x128>(v1[u]);
      }
      const unsigned qb = __float_as_uint(v1[1]);
      st64((u32x2){__float_as_uint(v1[0]), qb == ~0u ? 0x7fc00000u : qb}, rsl, fr < 8 ? tn * 256 + (8 * kg + fr) * 8 : 0x10000);
    }
    const rsrc_t rw3 = mkrsrc(ws + WS_W3T + (size_t)tc * 72 * 4608, 72 * 4608);
#define ISS3(s) issue_b(sm + so3(s), w, rw3, vl, ((s) + NYD) * 18432)
    bf16x8 RB[NXK][5];
    const unsigned b4 = 4096 + kg * 128 + (fr & 7) * 16;
#define RBL(i) do { _Pragma("unroll") for (int nt = 0; nt < 5; ++nt) \
      RB[i][nt] = __builtin_bit_cast(bf16x8, ld128(rw3, nt < 4 ? vl + nt * 1024 : b4, (72 - 4 * NXK + NXK * w + (i)) * 4608)); SCHED(); } while (0)
    __builtin_amdgcn_s_setprio(0);
    if (lane == 0) *(volatile LAS unsigned*)(sm + T_PROG + 16 + w * 4) = 1u;
    SCHED(); RBL(0); RBL(1); RBL(2);
    {
      u32x4 g[4];
      _Pragma("unroll 1") for (unsigned spins = 0; spins < SPIN_CAP; ++spins) {
        bool ok = true;
#pragma unroll
        for (int i = 0; i < 4; ++i) { g[i] = ld128(rsl, vl + i * 1024, 0); ok &= g[i][1] != ~0u && g[i][3] != ~0u; }
        if (__all(ok)) break;
        NAP(6);
      }
      __builtin_amdgcn_s_setprio(2);
      f32x4 t = (__builtin_bit_cast(f32x4, g[0]) + __builtin_bit_cast(f32x4, g[1])) + (__builtin_bit_cast(f32x4, g[2]) + __builtin_bit_cast(f32x4, g[3]));
      float s, qq;
      { const auto x = __builtin_amdgcn_permlane16_swap(__float_as_uint(t[0]), __float_as_uint(t[2]), false, false); s = __uint_as_float(x[0]) + __uint_as_float(x[1]); }
      { const auto x = __builtin_amdgcn_permlane16_swap(__float_as_uint(t[1]), __float_as_uint(t[3]), false, false); qq = __uint_as_float(x[0]) + __uint_as_float(x[1]); }
      { const auto x = __builtin_amdgcn_permlane32_swap(__float_as_uint(s), __float_as_uint(s), false, false); s = __uint_as_float(x[0]) + __uint_as_float(x[1]); }
      { const auto x = __builtin_amdgcn_permlane32_swap(__float_as_uint(qq), __float_as_uint(qq), false, false); qq = __uint_as_float(x[0]) + __uint_as_float(x[1]); }
      const float mean = s * (1.0f / FH);
      const f32x2 sc = ln_scalars(mean, qq * (1.0f / FH) - mean * mean);
      const float ri = __builtin_amdgcn_rcpf(sc.y);
      WFENCE();
      if (lane < 32) *(LAS f32x2*)(sm + T_LN3 + (w * 32 + 2 * fr + kg) * 8) = (f32x2){ri, -sc.x * ri};
      WFENCE();
#pragma unroll
      for (int mt = 0; mt < 2; ++mt) {
        const f32x4 t0 = *(const LAS f32x4*)(sm + T_LN3 + (w * 32 + 8 * kg + 4 * mt) * 8), t1 = *(const LAS f32x4*)(sm + T_LN3 + (w * 32 + 8 * kg + 4 * mt) * 8 + 16);
        rs4[mt] = (f32x4){t0[0], t0[2], t1[0], t1[2]}; nm4[mt] = (f32x4){t0[1], t0[3], t1[1], t1[3]};
      }
      const unsigned zo = vl + (4 * tm + w) * 9216;
#pragma unroll
      for (int nt = 0; nt < 9; ++nt) {
        float c[4], d[4];
#pragma unroll
        for (int j = 0; j < 4; ++j) {
          c[j] = __builtin_fmaf(acc[0][nt][j], rs4[0][j], nm4[0][j]); d[j] = __builtin_fmaf(acc[1][nt][j], rs4[1][j], nm4[1][j]);
          asm("" : "+v"(c[j])); asm("" : "+v"(d[j]));
        }
        st128((u32x4){cvtpk(c[0], c[1]), cvtpk(c[2], c[3]), cvtpk(d[0], d[1]), cvtpk(d[2], d[3])}, rz, zo + nt * 1024);
      }
    }
    WAITV(0);
    if (lane < 2) __hip_atomic_store((gu32*)(ws + WS_FLAGZ) + tn * 1024 + lane * 512 + tm * 32 + w, tag, RLX_AGENT);
    __builtin_amdgcn_s_setprio(0);
    const unsigned vs = (lane >> 3) * 4096 + (((lane & 7) ^ (lane >> 3)) << 4);
    if (lane == 0) *(volatile LAS unsigned*)(sm + T_PROG + 16 + w * 4) = 2u;
    ysync_wait(sm + 16, 2);
    ysync_wait(sm, 5); ISS2A(0); ysync_wait(sm, 6); ISS2A(1);
    if (w == 0) {
      _Pragma("unroll 1") for (unsigned spins = 0; spins < SPIN_CAP; ++spins) {
        const u32x4 f = ld128(mkrsrc(ws + WS_FLAGZ + tn * 4096 + (tm >> 3) * 2048, 2048), fr * 128, 0);
        if (__all(ALLTAG(f))) break;
        NAP(6);
      }
      if (lane == 0) *(volatile LAS unsigned*)(sm + T_PROG + 16) = 3u;
    } else {
      _Pragma("unroll 1") for (unsigned spins = 0; spins < LDS_SPIN_CAP && *(volatile LAS unsigned*)(sm + T_PROG + 16) != 3u; ++spins) NAP(1);
    }
    __builtin_amdgcn_fence(__ATOMIC_ACQUIRE, "wavefront");
    ISS2B(0); ISS2B(1); SCHED();
    ysync_wait(sm, 8); ISS2A(2); ISS2B(2);
    wait_parts<1, 2>(w); BAR(); ISS2A(3); ISS2B(3);
    wait_parts<2, 2>(w); BAR(); ISS2A(4); ISS2B(4);
    wait_parts<2, 2>(w); BAR(); ISS2A(5); ISS2B(5);
#define G2RUN(lo, hi) _Pragma("unroll 1") for (int g = (lo); g < (hi); ++g) { wait_parts<2, 2>(w); BAR(); ISS2A(g + 4); ISS2B(g + 4); }
    G2RUN(2, 8); RBL(3); G2RUN(8, 14); RBL(4); G2RUN(14, 20); RBL(5); G2RUN(20, 28);
    ISS3(7);
    wait_parts<2, 3>(w); BAR(); ISS3(0);
    wait_parts<1, 3>(w); BAR(); ISS3(1); ISS3(2);
    wait_parts<0, 4>(w); BAR(); ISS3(3);
    {
      const unsigned vt = (lane >> 3) * 256 + (((((t3 & 1) << 2) | ((lane & 7) >> 1)) ^ (lane >> 3)) << 5) + (lane & 1) * 16;
      const int st = tc * 72 * 256;
      const rsrc_t rh3 = mkrsrc(ws + WS_HT + (size_t)(t3 >> 1) * D_ * 256, D_ * 256);
      dma1k<1>(sm + T_H + w * 1024, rh3, vt, st + w * 2048);
      dma1k<1>(sm + T_H + (w + 4) * 1024, rh3, vt, st + (w + 4) * 2048);
      dma1k<1>(sm + T_H + 8 * 1024, rh3, vt, st + 8 * 2048);
    }
    BAR(); ISS3(4);
    BAR();
    ISS3(5); ISS3(6);
    WAITV(0); BAR();
    f32x4 cx[4][5];
    {
      const rsrc_t rg = mkrsrc(ws + WS_GZ + (size_t)t3 * 4 * 73728, 4 * 73728);
      bf16x8 G[NXK][4];
#define LGX(i) do { _Pragma("unroll") for (int m = 0; m < 4; ++m) \
        G[i][m] = __builtin_bit_cast(bf16x8, ld128(rg, vl, m * 73728 + (72 - 4 * NXK + NXK * w + (i)) * 1024)); SCHED(); } while (0)
#define MMGX(i) do { _Pragma("unroll") for (int m = 0; m < 4; ++m) _Pragma("unroll") for (int nt = 0; nt < 5; ++nt) \
        cx[m][nt] = __builtin_amdgcn_mfma_f32_16x16x32_bf16(RB[i][nt], G[i][m], (i) ? cx[m][nt] : (f32x4){0.f, 0.f, 0.f, 0.f}, 0, 0, 0); SCHED(); } while (0)
      static_assert(NXK == 9, "schedule below");
      LGX(0); LGX(1);
      MMGX(0); LGX(2); RBL(6);
      MMGX(1); LGX(3); RBL(7);
      MMGX(2); LGX(4); RBL(8);
      MMGX(3); LGX(5); LGX(6);
      MMGX(4); LGX(7); LGX(8);
      MMGX(5); MMGX(6); MMGX(7);
      ysync_wait(sm, 16);
      LAS char* o = sm + so3(0) + (w == 3 ? -4 : w) * 18432 + vl;
      LAS char* o4 = kg < 2 ? o + 4096 : sm + T_DEAD + (lane & 15) * 16;
#pragma unroll
      for (int m = 0; m < 4; ++m) {
#pragma unroll
        for (int nt = 0; nt < 5; ++nt) cx[m][nt] = __builtin_amdgcn_mfma_f32_16x16x32_bf16(RB[8][nt], G[8][m], cx[m][nt], 0, 0, 0);
#pragma unroll
        for (int nt = 0; nt < 4; ++nt) *(LAS f32x4*)(o + m * 4608 + nt * 1024) = cx[m][nt];
        *(LAS f32x4*)(o4 + (kg < 2 ? m * 4608 : 0)) = cx[m][4];
      }
      __builtin_amdgcn_sched_group_barrier(0x8, 5, 0);
#pragma unroll
      for (int q_ = 0; q_ < 15; ++q_) { __builtin_amdgcn_sched_group_barrier(0x8, 1, 0); __builtin_amdgcn_sched_group_barrier(0x200, 1, 0); }
    }
    WAITL(0);
    BAR();
    if (blockIdx.x == 0 && w == 0 && lane == 0) __hip_atomic_store((gu32*)(ws + WS_EPOCH), tag, RLX_AGENT);
    return;
  }
  LANE_CONSTS();
  const int aoff = krow * 256 + (((2 * w) ^ (krow & 7)) << 5) + (lane & 3) * 8;
  const rsrc_t rw1 = mkrsrc(ws + WS_W1Y + (size_t)tn * 9 * 18432, 9 * 18432);
  const rsrc_t rwz = mkrsrc(ws + WS_W1Z + (size_t)tn * 9 * 18432, 9 * 18432);
  u32x4 z1[9];
  u16 bs0, bs1;
  {
    bf16x8 HC[18][2];
#define CAP(s) do { RDAT(HC[2 * (s)], ((s) & 3) * STG, 0, 32); RDAT(HC[2 * (s) + 1], ((s) & 3) * STG, 1, 32); SCHED(); } while (0)
#define ZO(n) ((n) == 0 || (n) == 8 ? T_E : (n) < 6 ? ((n) + 1) * 18432 : ((n) - 6) * 18432)
#define ISSZ(n) issue_b(sm + ZO(n), w, rwz, vl, (n) * 18432)
    __builtin_amdgcn_s_waitcnt(0x0F70);
    BAR();
    ISS1B(0); ISS1B(1); ISS1B(2); ISS1B(3);
    LBAR();
    __builtin_amdgcn_s_setprio(3);
    ISS1A(0); ISS1A(1);
    wait_parts<1, 0>(w); BAR(); CAP(0); ISS1A(2); ISS1A(3);
    wait_parts<2, 0>(w); WAITL(0); BAR(); CAP(1); ISS1(4);
    wait_parts<2, 1>(w);
    wait_parts<2, 2>(w); WAITL(0); BAR(); CAP(2); ISS1(5);
    wait_parts<2, 2>(w); WAITL(0); BAR(); CAP(3);
    u32x4 f2;
    { const int q2 = 27 + min(lane, 44);
      asm volatile("s_nop 4\n\tbuffer_load_dwordx4 %0, %1, %2, 0 offen sc1" : "=v"(f2) : "v"((q2 >> 2) * 128 + (q2 & 3) * 16),
                   "s"(mkrsrc(ws + WS_FLAGA + (bb & 7) * 8192 + 36 * 128, 18 * 128)) : "memory"); }
    ISS1(6);
    wait_parts<2, 2>(w); WAITL(0); BAR(); CAP(4); ISS1(7);
    wait_parts<2, 2>(w); WAITL(0); BAR(); CAP(5); ISS1(8);
    wait_parts<2, 2>(w); WAITL(0); BAR(); CAP(6);
    asm volatile("" : "+v"(f2));
    if (!__all(ALLTAG(f2))) {
      _Pragma("unroll 1") for (unsigned spins = 0; spins < SPIN_CAP; ++spins) {
        const u32x4 f = wflags(ws, 27 + min(lane, 44));
        if (__all(ALLTAG(f))) break;
        NAP(8);
      }
    }
    ISSZ(0);
    if (w == 3) TOUCH_CODE();
    wait_parts<1, 2>(w); WAITL(0); BAR(); CAP(7); ISSZ(1);
    wait_parts<0, 2>(w); WAITL(0); BAR(); CAP(8);
    SCHED(); ISSZ(2);
    wait_parts<0, 2>(w); WAITL(0); BAR();
    __builtin_amdgcn_s_setprio(0);
    ISSZ(3); ISSZ(4); ISSZ(5); ISSZ(6); ISSZ(7);
    { constexpr int TR = (65536 - 1152 * TCONV + 1023) / 1024;
      touch(sm, mkrsrc(a.wsp, L_ * L_ * 2), lane < TR ? vl * 8 : 0x1000000, (1152 * TCONV + (bb * 4 + w) * TR) * 128); }
#define POST(k) do { WAITL(0); if (lane == 0) *(volatile LAS unsigned*)(sm + T_PROG + w * 4) = (k); } while (0)
    bf16x8 W[18];
    f32x4 c0[2], c1[2];
    constexpr int ZW = 5;
#define RDZ(n, lo, hi) do { _Pragma("unroll") for (int ks = (lo); ks < (hi); ++ks) W[ks] = *(const LAS bf16x8*)(sm + ZO(n) + vl + ks * 1024); } while (0)
#define MMZ(c, lo, hi) do { _Pragma("unroll") for (int ks = (lo); ks < (hi); ++ks) _Pragma("unroll") for (int mt = 0; mt < 2; ++mt) \
      c[mt] = __builtin_amdgcn_mfma_f32_16x16x32_bf16(W[ks], HC[ks][mt], ks ? c[mt] : (f32x4){0.f, 0.f, 0.f, 0.f}, 0, 0, 0); } while (0)
#define CS4(n) c4 = *(const LAS f32x4*)(sm + T_CS + (144 + (n) * 16 + kg * 4) * 4)
#define GELA(c) do { _Pragma("unroll") for (int j = 0; j < 4; ++j) { \
        const f32x2 v_ = {c[j >> 1][2 * (j & 1)], c[j >> 1][2 * (j & 1) + 1]}, k_ = {c4[2 * (j & 1)], c4[2 * (j & 1) + 1]}; \
        gx[j] = v_ * rn[j >> 1].x + k_ * rn[j >> 1].y; \
        ga[j] = gx[j] * ((gx[j] * gx[j]) * (-2.3022082f * 0.044715f) + (-2.3022082f)); \
        asm volatile("" : "+v"(gx[j]), "+v"(ga[j])); } } while (0)
#define GELB(n) do { float e_[8]; \
      _Pragma("unroll") for (int j = 0; j < 8; ++j) e_[j] = __builtin_amdgcn_exp2f(ga[j >> 1][j & 1]); \
      _Pragma("unroll") for (int j = 0; j < 8; ++j) { e_[j] += 1.0f; asm("" : "+v"(e_[j])); } \
      _Pragma("unroll") for (int j = 0; j < 8; ++j) e_[j] = __builtin_amdgcn_rcpf(e_[j]); \
      _Pragma("unroll") for (int j = 0; j < 8; ++j) { e_[j] *= gx[j >> 1][j & 1]; asm("" : "+v"(e_[j])); } \
      _Pragma("unroll") for (int j = 0; j < 4; ++j) z1[n][j] = cvtpk(e_[2 * j], e_[2 * j + 1]); } while (0)
    f32x2 gx[4], ga[4], rn[2];
    f32x4 c4;
#define ILVZ(NG, NV) do { _Pragma("unroll") for (int q_ = 0; q_ < (NG); ++q_) { __builtin_amdgcn_sched_group_barrier(0x8, 2, 0); \
      __builtin_amdgcn_sched_group_barrier(0x100, 1, 0); if (NV) __builtin_amdgcn_sched_group_barrier(0x402, NV, 0); } } while (0)
#define ILVZ1(NG, NV) do { _Pragma("unroll") for (int q_ = 0; q_ < (NG); ++q_) { __builtin_amdgcn_sched_group_barrier(0x8, 1, 0); __builtin_amdgcn_sched_group_barrier(0x402, NV, 0); \
      __builtin_amdgcn_sched_group_barrier(0x8, 1, 0); __builtin_amdgcn_sched_group_barrier(0x100, 1, 0); __builtin_amdgcn_sched_group_barrier(0x402, NV, 0); } } while (0)
#define TILE(n, c, cp) do { \
      if ((n) == 6) WAITV(0); if ((n) == 7) ysync_wait(sm, 6); \
      if (lane == 0) *(volatile LAS unsigned*)(sm + T_PROG + w * 4) = (n); GELA(cp); SCHED(); \
      RDZ(n, ZW, 18); MMZ(c, 0, 18); CS4(n); if ((n) < 8) RDZ((n) + 1, 0, ZW); GELB((n) - 1); ILVZ1(18, 1); asm volatile("" : "+v"(z1[(n) - 1])); SCHED(); } while (0)
    RDZ(0, 0, 6); SCHED();
    RDZ(0, 6, 18); MMZ(c0, 0, 12); ILVZ(12, 0); SCHED(); MMZ(c0, 12, 18); SCHED();
    WAITV(1);
    POST(1); ysync_wait(sm, 1);
    ISSZ(8);
    bs0 = a.bsp[tm * 128 + w * 32 + fr]; bs1 = a.bsp[tm * 128 + w * 32 + 16 + fr]; SCHED();
    RDZ(1, 0, ZW); CS4(0);
    _Pragma("unroll 1") for (unsigned spins = 0; spins < LDS_SPIN_CAP && *(volatile LAS unsigned*)(sm + T_PROG + 16 + w * 4) == 0u; ++spins) NAP(2);
    asm volatile("" ::: "memory");
#pragma unroll
    for (int mt = 0; mt < 2; ++mt) rn[mt] = *(const LAS f32x2*)(sm + T_LN2 + (w * 32 + mt * 16 + fr) * 8);
    SCHED();
    TILE(1, c1, c0); TILE(2, c0, c1); TILE(3, c1, c0); TILE(4, c0, c1); TILE(5, c1, c0); TILE(6, c0, c1); TILE(7, c1, c0); TILE(8, c0, c1);
    GELA(c0); GELB(8);
  }
  const float b0 = bf2f(bs0), b1 = bf2f(bs1);
  {
    const int aoff2 = (w * 32 + fr) * 128 + ((kg ^ (lane & 7)) << 4);
#define ITER(slot, LAST) do { \
    RDA(F1, (slot) * STG, 1); RDB(F1, (slot) * STG, 1); MMY(F0); ILV(11); SCHED(); \
    WAITL(0); BAR(); SCHED(); \
    if (!(LAST)) { RDA(F0, (((slot) + 1) & 3) * STG, 0); RDB(F0, (((slot) + 1) & 3) * STG, 0); } \
    MMY(F1); if (!(LAST)) ILV(11); SCHED(); } while (0)
#pragma unroll
    for (int nt = 0; nt < 9; ++nt) { acc[0][nt] = (f32x4){b0, b0, b0, b0}; acc[1][nt] = (f32x4){b1, b1, b1, b1}; }
    BAR();
    RDA(F0, 2 * STG, 0); RDB(F0, 2 * STG, 0);
    for (int g = 0; g < 28; g += 4) { ITER(2, false); ITER(3, false); ITER(0, false); ITER(1, false); }
    ITER(2, false); ITER(3, false); ITER(0, false); ITER(1, true);
#undef ITER
  }
  const rsrc_t rg = mkrsrc(ws + WS_GZ + (size_t)tm * 8 * 73728, 8 * 73728);
  {
    const int k0 = 9 * (tn >> 1);
    unsigned o8[2][2];
#pragma unroll
    for (int mt = 0; mt < 2; ++mt) {
      unsigned o[18];
#pragma unroll
      for (int nt = 0; nt < 9; ++nt)
#pragma unroll
        for (int j = 0; j < 2; ++j) {
          const unsigned z = z1[nt][2 * mt + j];
          o[2 * nt + j] = cvtpk(bflo(z) * acc[mt][nt][2 * j], bfhi(z) * acc[mt][nt][2 * j + 1]);
        }
      const int sb = (2 * w + mt) * 73728 + k0 * 1024;
#pragma unroll
      for (int i = 0; i < 4; ++i) st128((u32x4){o[4 * i], o[4 * i + 1], o[4 * i + 2], o[4 * i + 3]}, rg, vl + sb + (tn & 1) * 4096 + i * 1024);
      o8[mt][0] = o[16]; o8[mt][1] = o[17];
    }
    const auto s0 = __builtin_amdgcn_permlane16_swap(o8[0][0], o8[1][0], false, false);
    const auto s1 = __builtin_amdgcn_permlane16_swap(o8[0][1], o8[1][1], false, false);
    st128((u32x4){s0[0], s1[0], s0[1], s1[1]}, rg, (2 * w + (kg & 1)) * 73728 + k0 * 1024 + 8192 + (2 * (tn & 1) + (kg >> 1)) * 256 + fr * 16);
  }
  SCHED();
  WAITV(0);
  if (lane == 0) __hip_atomic_store((gu32*)(ws + WS_FLAGG) + (tm * 4 + w) * 128 + (tn >> 2) * 32 + (tn & 3), tag, RLX_AGENT);
  SCHED(); BAR();
  const int rh2 = w & 1, kh = w >> 1, rp = 2 * t3 + rh2;
  bf16x8 B0[10], B1[10];
  const int bl = vl + kh * 9216, b4 = 4096 + kg * 128 + (fr & 7) * 16 + kh * 9216;
  {
    const rsrc_t rw3 = mkrsrc(ws + WS_W3T + (size_t)tc * 72 * 4608, 72 * 4608);
#define LD3(B, st) do { _Pragma("unroll") for (int kk = 0; kk < 2; ++kk) _Pragma("unroll") for (int nt = 0; nt < 5; ++nt) \
      B[kk * 5 + nt] = __builtin_bit_cast(bf16x8, ld128(rw3, nt < 4 ? bl + nt * 1024 : b4, (st) * 18432 + kk * 4608)); } while (0)
    LD3(B0, 0);
    if (NYD > 1) LD3(B1, 1);
    SCHED();
  }
  NAP(12);
  _Pragma("unroll 1") for (unsigned spins = 0; spins < SPIN_CAP; ++spins) {
    const u32x4 f = ld128(mkrsrc(ws + WS_FLAGG + rp * 512, 512), (lane & 3) * 128, 0);
    if (__all(ALLTAG(f))) break;
    NAP(3);
  }
  __builtin_amdgcn_fence(__ATOMIC_ACQUIRE, "wavefront");
  {
    constexpr int NY = 8 + NYD;
    f32x4 c3[2][5] = {};
    bf16x8 G[6][2][2];
    const rsrc_t rg0 = mkrsrc(ws + WS_GZ + (size_t)(2 * rp) * 73728, 73728), rg1 = mkrsrc(ws + WS_GZ + (size_t)(2 * rp + 1) * 73728, 73728);
    const unsigned vg = vl + kh * 2048;
#define LDG(u, st) do { __builtin_amdgcn_s_setprio(1); _Pragma("unroll") for (int kk = 0; kk < 2; ++kk) { G[u][kk][0] = __builtin_bit_cast(bf16x8, ld128(rg0, vg + kk * 1024, (st) * 4096)); \
      G[u][kk][1] = __builtin_bit_cast(bf16x8, ld128(rg1, vg + kk * 1024, (st) * 4096)); } SCHED(); __builtin_amdgcn_s_setprio(0); } while (0)
    LDG(0, 0); SCHED();
    BAR();
#pragma unroll
    for (int u = 1; u < GAH; ++u) LDG(u, u);
#define RD3(B, so) do { _Pragma("unroll") for (int kk = 0; kk < 2; ++kk) { \
      _Pragma("unroll") for (int nt = 0; nt < 4; ++nt) B[kk * 5 + nt] = *(const LAS bf16x8*)(sm + (so) + bl + kk * 4608 + nt * 1024); \
      B[kk * 5 + 4] = *(const LAS bf16x8*)(sm + (so) + b4 + kk * 4608); } } while (0)
#define MM3(B, u) do { _Pragma("unroll") for (int kk = 0; kk < 2; ++kk) _Pragma("unroll") for (int nt = 0; nt < 5; ++nt) _Pragma("unroll") for (int m = 0; m < 2; ++m) \
      c3[m][nt] = __builtin_amdgcn_mfma_f32_16x16x32_bf16(B[kk * 5 + nt], G[u][kk][m], c3[m][nt], 0, 0, 0); } while (0)
#define STAGE3(s, BC, BN) do { if ((s) < NY) { \
    if (((s) == NYD + 4 || (s) == NYD + 6) && lane == 0) *(volatile LAS unsigned*)(sm + T_PROG + w * 4) = (s) == NYD + 4 ? 16u : 17u;     \
    if ((s) + 1 >= NYD && (s) + 1 < NY) { RD3(BN, so3((s) + 1 - NYD)); MM3(BC, (s) % 6); ILV2(0, 10, 20); } else MM3(BC, (s) % 6); SCHED(); \
    if ((s) + GAH < NY) LDG(((s) + GAH) % 6, (s) + GAH); SCHED(); } } while (0)
    SCHED();
    STAGE3(0, B0, B1); STAGE3(1, B1, B0); STAGE3(2, B0, B1); STAGE3(3, B1, B0); STAGE3(4, B0, B1); STAGE3(5, B1, B0);
    STAGE3(6, B0, B1); STAGE3(7, B1, B0); STAGE3(8, B0, B1); STAGE3(9, B1, B0);
#undef STAGE3
#undef LDG
    const int tt = 2 * rh2 + kh;
    ysync_wait(sm, 17);
#pragma unroll
    for (int nt = 0; nt < 5; ++nt) *(LAS f32x4*)(sm + 18432 + 1024 + (w * 5 + nt) * 1024 + vl) = kh ? c3[0][nt] : c3[1][nt];
    f32x4 cs4[5];
    LAS const char* hp = sm + T_H + (4 * kg + (fr >> 2)) * 128 + tt * 32 + (lane & 3) * 8;
#pragma unroll
    for (int nt = 0; nt < 5; ++nt) {
      const u32x2 h = __builtin_bit_cast(u32x2, trread(hp + nt * 2048));
      cs4[nt] = (kh ? c3[1][nt] : c3[0][nt]) + (f32x4){bflo(h[0]), bfhi(h[0]), bflo(h[1]), bfhi(h[1])};
    }
    LBAR();
    u16* op = a.out + (size_t)(t3 * 64 + tt * 16 + fr) * D_ + tc * 72 + kg * 4;
#pragma unroll
    for (int nt = 0; nt < 5; ++nt) {
      LAS const char* xp = sm + tt * 4608 + nt * 1024 + vl;
      const f32x4 c = (cs4[nt] + *(const LAS f32x4*)(sm + 18432 + 1024 + ((w ^ 2) * 5 + nt) * 1024 + vl)) +
                      ((*(const LAS f32x4*)(xp + 4 * 18432) + *(const LAS f32x4*)(xp + 5 * 18432)) + (*(const LAS f32x4*)(xp + 6 * 18432) + *(const LAS f32x4*)(xp)));
      const u32x2 o = {cvtpk(c[0], c[1]), cvtpk(c[2], c[3])};
      if (nt < 4 || kg < 2) *(u32x2*)(op + nt * 16) = o;
    }
  }
}

extern "C" void kernel_launch(void* const* d_in, const int* in_sizes, int n_in, void* d_out, int out_size, void* d_ws,
                              size_t ws_size, hipStream_t stream) {
  Args a;
  a.x = (const u16*)d_in[0]; a.are = (const u16*)d_in[1]; a.aim = (const u16*)d_in[2]; a.bre = (const u16*)d_in[3];
  a.bim = (const u16*)d_in[4]; a.cre = (const u16*)d_in[5]; a.cim = (const u16*)d_in[6]; a.logdt = (const u16*)d_in[7];
  a.dskip = (const u16*)d_in[8]; a.win = (const u16*)d_in[10]; a.wsp = (const u16*)d_in[11]; a.bsp = (const u16*)d_in[12];
  a.wout = (const u16*)d_in[13]; a.out = (u16*)d_out; a.ws = (char*)d_ws;
  (void)hipFuncSetAttribute((const void*)fused_block, hipFuncAttributeMaxDynamicSharedMemorySize, LDS_BYTES);
  fused_block<<<256, 512, LDS_BYTES, stream>>>(a);
}
```
